```python
import torch

def _softmax(x, axis=-1):
    return torch.softmax(x, dim=axis)

def _gelu(x):
    return torch.nn.functional.gelu(x, approximate='tanh')

def _silu(x):
    return torch.nn.functional.silu(x)

def _sigmoid(x):
    return torch.sigmoid(x)

def _layernorm(x, axis=-1, eps=1e-05):
    m = x.mean(dim=axis, keepdim=True)
    v = x.var(dim=axis, keepdim=True, unbiased=False)
    return (x - m) / torch.sqrt(v + eps)

def _scatter_add(tgt, idx, src, axis=0):
    return tgt.clone().index_add_(axis, idx, src)

def _segment_sum(src, seg, n):
    out = torch.zeros((n,) + src.shape[1:], dtype=src.dtype)
    return out.index_add_(0, seg, src)

def _pad(x, pw):
    flat = tuple((v for pair in reversed(pw) for v in pair))
    return torch.nn.functional.pad(x, flat)
kernel_name = 'standalone__autoregressive_conv_wavernn__speech'

def setup_inputs():

    _w = lambda a: (lambda t: t.to(torch.bfloat16) if t.is_floating_point() else t)(torch.as_tensor(a))
    B, T, D, K = (2, 1000, 512, 15)
    return {'x': _w((lambda *s: torch.randn(*s, dtype=torch.bfloat16) * 0.05)(B, T, D)), 'w': _w((lambda *s: torch.randn(*s, dtype=torch.bfloat16) * 0.05)(K, D, 2 * D))}

def reference(x, w):
    B, T, D = x.shape
    K = w.shape[0]
    dil = 2
    pad = (K - 1) * dil
    xp = _pad(x, ((0, 0), (pad, 0), (0, 0)))
    h = torch.einsum('btd,de->bte', xp[:, 0:T, :], w[0])
    for i in range(1, K):
        h = h + torch.einsum('btd,de->bte', xp[:, i * dil:i * dil + T, :], w[i])
    f, g = (h[..., :D].clone(), h[..., D:].clone())
    return torch.tanh(f) * _sigmoid(g)
```

```cpp
#include <hip/hip_runtime.h>
#include <hip/hip_bf16.h>
#include <stdint.h>

__global__ __launch_bounds__(256, 1) void conv_gate_kernel(const __hip_bfloat16* __restrict__ x,
                                                           const __hip_bfloat16* __restrict__ w,
                                                           __hip_bfloat16* __restrict__ out,
                                                           unsigned* __restrict__ ws) {
  __shared__ __attribute__((aligned(1024))) char lds[163840];
  const unsigned ldsb = (unsigned)(size_t)lds;
  asm volatile(
      "s_load_dwordx8 s[8:15], %0, 0\n\t"
      "s_getpc_b64 s[32:33]\n\t"
      "Lcs%=:\n\t"
      "v_lshrrev_b32 v244, 6, %2\n\t"
      "v_and_b32 v1, 63, %2\n\t"
      "v_readfirstlane_b32 s36, v244\n\t"
      "s_and_b32 s38, %1, 7\n\t"
      "s_lshr_b32 s56, %1, 3\n\t"
      "s_and_b32 s39, s56, 1\n\t"
      "s_xor_b32 s38, s38, s39\n\t"
      "v_lshrrev_b32 v244, 2, v1\n\t"
      "v_and_b32 v245, 1, v1\n\t"
      "v_and_b32 v249, 2, v1\n\t"
      "s_lshr_b32 s41, s56, 1\n\t"
      "s_lshl_b32 s41, s41, 2\n\t"
      "s_add_u32 s41, s41, s36\n\t"
      "s_lshl_b32 s41, s41, 12\n\t"
      "s_lshl_b32 s42, s38, 7\n\t"
      "s_lshl_b32 s59, s39, 10\n\t"
      "s_or_b32 s42, s42, s59\n\t"
      "s_or_b32 s41, s41, s42\n\t"
      "v_lshlrev_b32 v247, 20, v244\n\t"
      "v_mul_u32_u24 v248, 1152, v245\n\t"
      "v_lshl_or_b32 v247, v249, 10, v247\n\t"
      "v_or_b32 v247, s41, v247\n\t"
      "v_xor_b32 v33, v248, v247\n\t"
      "s_mov_b32 s94, 0x33333333\n\t"
      "s_mov_b32 s95, 0x3333\n\t"
      "s_lshl_b32 s41, s39, 1\n\t"
      "s_lshl_b64 s[94:95], s[94:95], s41\n\t"
      "s_mul_i32 s37, s36, 40960\n\t"
      "s_add_u32 s37, s37, %3\n\t"
      "s_bfe_u32 s41, s56, 131073\n\t"
      "s_bfe_u32 s43, s56, 65539\n\t"
      "s_mul_i32 s58, s41, 250\n\t"
      "s_add_u32 s58, s58, s43\n\t"
      "s_lshr_b32 s43, s56, 4\n\t"
      "s_mul_i32 s43, s43, 1024000\n\t"
      "s_lshl_b32 s55, s58, 10\n\t"
      "s_mul_i32 s41, s36, 1984\n\t"
      "s_sub_u32 s55, s55, s41\n\t"
      "s_sub_u32 s55, s55, 28672\n\t"
      "v_lshrrev_b32 v245, 5, v1\n\t"
      "v_and_b32 v246, 3, v1\n\t"
      "v_xor_b32 v244, v244, v245\n\t"
      "v_lshlrev_b32 v247, 4, v246\n\t"
      "v_lshl_or_b32 v244, v244, 11, v247\n\t"
      "v_xor_b32 v245, 0x1000, v244\n\t"
      "v_xor_b32 v247, 32, v244\n\t"
      "v_xor_b32 v248, 32, v245\n\t"
      "v_add_u32 v22, s55, v244\n\t"
      "s_add_u32 s41, s55, 32768\n\t"
      "v_add_u32 v27, s41, v245\n\t"
      "s_add_u32 s41, s55, 65536\n\t"
      "v_add_u32 v23, s41, v247\n\t"
      "s_add_u32 s41, s55, 97280\n\t"
      "v_add_u32 v24, s41, v248\n\t"
      "v_add_u32 v25, 129024, v22\n\t"
      "v_add_u32 v28, 131072, v27\n\t"
      "v_add_u32 v29, 130048, v23\n\t"
      "v_add_u32 v30, 130048, v24\n\t"
      "v_add_u32 v26, 262144, v22\n\t"
      "s_cmp_eq_u32 s39, 0\n\t"
      "s_cbranch_scc1 Lnsw%=\n\t"
      "v_swap_b32 v22, v27\n\t"
      "v_swap_b32 v23, v28\n\t"
      "v_swap_b32 v24, v29\n\t"
      "v_swap_b32 v25, v30\n\t"
      "Lnsw%=:\n\t"
      "s_mul_i32 s100, s39, 3072\n\t"
      "s_add_u32 s59, s59, s37\n\t"
      "s_add_u32 s41, s37, 2048\n\t"
      "s_add_u32 s100, s100, s41\n\t"
      "s_mov_b32 s18, 1024000\n\t"
      "s_mov_b32 s19, 0x00020000\n\t"
      "s_mov_b32 s22, 15728640\n\t"
      "s_mov_b32 s23, 0x00020000\n\t"
      "s_mov_b32 s57, 0\n\t"
      "s_mov_b32 m0, s59\n\t"
      "s_mov_b64 exec, s[94:95]\n\t"
      "s_waitcnt lgkmcnt(0)\n\t"
      "s_mov_b64 s[20:21], s[10:11]\n\t"
      "buffer_load_dword v35, v33, s[20:23], s57 offen\n\t"
      "s_mov_b64 exec, -1\n\t"
      "s_add_u32 s16, s8, s43\n\t"
      "s_addc_u32 s17, s9, 0\n\t"
      "buffer_load_dwordx4 v22, s[16:19], 0 offen offset:0 lds\n\t"
      "s_mov_b32 m0, s100\n\t"
      "s_nop 0\n\t"
      "buffer_load_dwordx4 v23, s[16:19], 0 offen offset:0 lds\n\t"
      "buffer_load_dwordx4 v24, s[16:19], 0 offen offset:1024 lds\n\t"
      "buffer_load_dwordx4 v25, s[16:19], 0 offen offset:2048 lds\n\t"
      "s_add_u32 m0, s37, 8192\n\t"
      "s_nop 0\n\t"
      "buffer_load_dwordx4 v26, s[16:19], 0 offen offset:0 lds\n\t"
      "v_and_b32 v244, 7, v1\n\t"
      "v_and_b32 v245, 16, v1\n\t"
      "v_lshlrev_b32 v244, 4, v244\n\t"
      "v_lshlrev_b32 v245, 1, v245\n\t"
      "s_lshl_b32 s41, s39, 6\n\t"
      "s_or_b32 s41, s41, s42\n\t"
      "s_lshl_b32 s42, s36, 16\n\t"
      "s_or_b32 s41, s41, s42\n\t"
      "v_xor_b32 v244, v244, v245\n\t"
      "v_and_b32 v245, 0x38, v1\n\t"
      "v_xor_b32 v244, s41, v244\n\t"
      "v_lshl_or_b32 v2, v245, 8, v244\n\t"
      "s_xor_b32 s41, s39, 1\n\t"
      "v_xor_b32 v244, 64, v2\n\t"
      "s_lshl_b32 s101, s41, 10\n\t"
      "v_add_u32 v4, 30720, v2\n\t"
      "s_add_u32 s101, s101, s37\n\t"
      "v_add_u32 v3, 15360, v244\n\t"
      "s_mul_i32 s92, s41, 3072\n\t"
      "v_add_u32 v5, 46080, v244\n\t"
      "s_add_u32 s92, s92, s37\n\t"
      "s_add_u32 s92, s92, 2048\n\t"
      "s_add_u32 m0, s37, 9216\n\t"
      "s_mov_b32 s40, 0\n\t"
      "buffer_load_dwordx4 v2, s[20:23], s40 offen offset:0 lds\n\t"
      "buffer_load_dwordx4 v3, s[20:23], s40 offen offset:1024 lds\n\t"
      "buffer_load_dwordx4 v4, s[20:23], s40 offen offset:2048 lds\n\t"
      "buffer_load_dwordx4 v5, s[20:23], s40 offen offset:3072 lds\n\t"
      "s_mov_b32 m0, s101\n\t"
      "s_nop 0\n\t"
      "buffer_load_dwordx4 v27, s[16:19], 0 offen offset:0 lds\n\t"
      "s_mov_b32 m0, s92\n\t"
      "s_nop 0\n\t"
      "buffer_load_dwordx4 v28, s[16:19], 0 offen offset:0 lds\n\t"
      "buffer_load_dwordx4 v29, s[16:19], 0 offen offset:1024 lds\n\t"
      "buffer_load_dwordx4 v30, s[16:19], 0 offen offset:2048 lds\n\t"
      "s_add_u32 s24, s12, s43\n\t"
      "s_addc_u32 s25, s13, 0\n\t"
      "s_mov_b64 s[28:29], s[14:15]\n\t"
      "s_mov_b32 s26, 1024000\n\t"
      "s_mov_b32 s27, 0x00020000\n\t"
      "s_mov_b32 s30, 16777216\n\t"
      "s_mov_b32 s31, 0x00020000\n\t"
      "s_mov_b32 s41, 0x33333333\n\t"
      "s_cmp_eq_u32 s39, 1\n\t"
      "s_cmov_b32 s41, 0xcccccccc\n\t"
      "s_cmp_eq_u32 s38, 3\n\t"
      "s_cmov_b32 s41, 0xaaaaaaaa\n\t"
      "s_cmp_eq_u32 s38, 2\n\t"
      "s_cmov_b32 s41, 0xaaaaaaaa\n\t"
      "s_mov_b32 vcc_lo, s41\n\t"
      "s_mov_b32 vcc_hi, s41\n\t"
      "v_bfrev_b32 v249, 1\n\t"
      "v_lshlrev_b32 v31, 4, v1\n\t"
      "v_cndmask_b32 v33, v249, v33, vcc\n\t"
      "s_movk_i32 s52, 256\n\t"
      "s_movk_i32 s53, 512\n\t"
      "s_movk_i32 s54, 768\n\t"
      "s_lshl_b32 s41, %1, 2\n\t"
      "s_add_u32 s41, s41, s36\n\t"
      "s_lshl_b32 s44, s41, 11\n\t"
      "s_xor_b32 s45, s44, 73728\n\t"
      "s_lshl_b32 s46, s41, 7\n\t"
      "s_add_u32 s46, s46, 4194304\n\t"
      "s_xor_b32 s47, s46, 4608\n\t"
      "v_add_u32 v32, s37, v31\n\t"
      "v_and_b32 v244, 0x30, v1\n\t"
      "v_and_b32 v245, 0x0c, v1\n\t"
      "v_and_b32 v247, 0x18, v1\n\t"
      "v_lshlrev_b32 v248, 3, v246\n\t"
      "v_lshl_or_b32 v248, v244, 6, v248\n\t"
      "v_lshl_or_b32 v248, v245, 5, v248\n\t"
      "v_lshl_or_b32 v248, v247, 2, v248\n\t"
      "v_add_u32 v6, s37, v248\n\t"
      "v_xor_b32 v7, 32, v6\n\t"
      "v_xor_b32 v8, 64, v6\n\t"
      "v_xor_b32 v9, 96, v6\n\t"
      "v_and_b32 v245, 15, v1\n\t"
      "v_add_u32 v244, s37, v244\n\t"
      "v_and_b32 v247, 3, v245\n\t"
      "v_and_b32 v248, 4, v245\n\t"
      "v_lshl_add_u32 v249, v245, 9, v244\n\t"
      "v_lshlrev_b32 v248, 3, v248\n\t"
      "v_lshl_add_u32 v249, v247, 6, v249\n\t"
      "v_xor_b32 v10, v248, v249\n\t"
      "v_xor_b32 v11, 64, v10\n\t"
      "v_xor_b32 v12, 128, v10\n\t"
      "v_xor_b32 v13, 192, v10\n\t"
      "v_add_u32 v245, 1, v245\n\t"
      "v_and_b32 v247, 3, v245\n\t"
      "v_and_b32 v248, 4, v245\n\t"
      "v_lshl_add_u32 v249, v245, 9, v244\n\t"
      "v_lshlrev_b32 v248, 3, v248\n\t"
      "v_lshl_add_u32 v249, v247, 6, v249\n\t"
      "v_xor_b32 v14, v248, v249\n\t"
      "v_xor_b32 v15, 64, v14\n\t"
      "v_xor_b32 v16, 128, v14\n\t"
      "v_xor_b32 v17, 192, v14\n\t"
      "v_add_u32 v245, 1, v245\n\t"
      "v_and_b32 v247, 3, v245\n\t"
      "v_and_b32 v248, 4, v245\n\t"
      "v_lshl_add_u32 v249, v245, 9, v244\n\t"
      "v_lshlrev_b32 v248, 3, v248\n\t"
      "v_lshl_add_u32 v249, v247, 6, v249\n\t"
      "v_xor_b32 v18, v248, v249\n\t"
      "v_xor_b32 v19, 64, v18\n\t"
      "v_xor_b32 v20, 128, v18\n\t"
      "v_xor_b32 v21, 192, v18\n\t"
      "s_cmp_eq_u32 s39, 0\n\t"
      "s_mov_b32 s48, 0xbfb8aa3b\n\t"
      "s_cmov_b32 s48, 0xc038aa3b\n\t"
      "s_mov_b32 s49, 0x47000000\n\t"
      "s_cmov_b32 s49, 0x47800000\n\t"
      "s_cselect_b32 s41, 0xc7000000, 0\n\t"
      "v_mov_b32 v41, s41\n\t"
      "s_mov_b32 s50, 0\n\t"
      "s_mov_b32 s51, 0\n\t"
      "s_add_u32 m0, s37, 13312\n\t"
      "s_mov_b32 s40, 1048576\n\t"
      "buffer_load_dwordx4 v2, s[20:23], s40 offen offset:0 lds\n\t"
      "buffer_load_dwordx4 v3, s[20:23], s40 offen offset:1024 lds\n\t"
      "buffer_load_dwordx4 v4, s[20:23], s40 offen offset:2048 lds\n\t"
      "buffer_load_dwordx4 v5, s[20:23], s40 offen offset:3072 lds\n\t"
      "s_add_u32 m0, s37, 17408\n\t"
      "s_mov_b32 s40, 2097152\n\t"
      "buffer_load_dwordx4 v2, s[20:23], s40 offen offset:0 lds\n\t"
      "buffer_load_dwordx4 v3, s[20:23], s40 offen offset:1024 lds\n\t"
      "buffer_load_dwordx4 v4, s[20:23], s40 offen offset:2048 lds\n\t"
      "buffer_load_dwordx4 v5, s[20:23], s40 offen offset:3072 lds\n\t"
      "s_waitcnt vmcnt(12)\n\t"
      "ds_read_b64_tr_b16 v[164:165], v6 offset:9216\n\t"
      "ds_read_b64_tr_b16 v[166:167], v6 offset:9728\n\t"
      "ds_read_b64_tr_b16 v[168:169], v7 offset:9216\n\t"
      "ds_read_b64_tr_b16 v[170:171], v7 offset:9728\n\t"
      "ds_read_b64_tr_b16 v[172:173], v8 offset:9216\n\t"
      "ds_read_b64_tr_b16 v[174:175], v8 offset:9728\n\t"
      "ds_read_b64_tr_b16 v[176:177], v9 offset:9216\n\t"
      "ds_read_b64_tr_b16 v[178:179], v9 offset:9728\n\t"
      "s_waitcnt vmcnt(8)\n\t"
      "ds_read_b128 v[76:79], v10 offset:0\n\t"
      "ds_read_b128 v[80:83], v11 offset:0\n\t"
      "ds_read_b128 v[84:87], v12 offset:0\n\t"
      "ds_read_b128 v[88:91], v13 offset:0\n\t"
      "ds_read_b128 v[92:95], v10 offset:256\n\t"
      "ds_read_b128 v[96:99], v11 offset:256\n\t"
      "ds_read_b128 v[100:103], v12 offset:256\n\t"
      "ds_read_b128 v[104:107], v13 offset:256\n\t"
      "s_waitcnt lgkmcnt(7)\n\t"
      "v_mfma_f32_16x16x32_bf16 a[0:3], v[164:167], v[76:79], 0\n\t"
      "ds_read_b128 v[108:111], v14 offset:0\n\t"
      "ds_read_b128 v[112:115], v15 offset:0\n\t"
      "s_waitcnt lgkmcnt(8)\n\t"
      "v_mfma_f32_16x16x32_bf16 a[32:35], v[164:167], v[80:83], 0\n\t"
      "ds_read_b128 v[116:119], v16 offset:0\n\t"
      "ds_read_b128 v[120:123], v17 offset:0\n\t"
      "s_waitcnt lgkmcnt(9)\n\t"
      "v_mfma_f32_16x16x32_bf16 a[64:67], v[164:167], v[84:87], 0\n\t"
      "ds_read_b128 v[124:127], v14 offset:256\n\t"
      "ds_read_b128 v[128:131], v15 offset:256\n\t"
      "s_waitcnt lgkmcnt(10)\n\t"
      "v_mfma_f32_16x16x32_bf16 v[44:47], v[164:167], v[88:91], 0\n\t"
      "ds_read_b128 v[132:135], v16 offset:256\n\t"
      "ds_read_b128 v[136:139], v17 offset:256\n\t"
      "s_waitcnt lgkmcnt(11)\n\t"
      "v_mfma_f32_16x16x32_bf16 a[16:19], v[164:167], v[92:95], 0\n\t"
      "ds_read_b128 v[140:143], v18 offset:0\n\t"
      "ds_read_b128 v[144:147], v19 offset:0\n\t"
      "s_waitcnt lgkmcnt(10)\n\t"
      "v_mfma_f32_16x16x32_bf16 a[48:51], v[164:167], v[96:99], 0\n\t"
      "ds_read_b128 v[148:151], v20 offset:0\n\t"
      "ds_read_b128 v[152:155], v21 offset:0\n\t"
      "v_mfma_f32_16x16x32_bf16 a[80:83], v[164:167], v[100:103], 0\n\t"
      "ds_read_b128 v[156:159], v18 offset:256\n\t"
      "ds_read_b128 v[160:163], v19 offset:256\n\t"
      "v_mfma_f32_16x16x32_bf16 v[60:63], v[164:167], v[104:107], 0\n\t"
      "s_add_u32 m0, s37, 21504\n\t"
      "s_mov_b32 s40, 3145728\n\t"
      "buffer_load_dwordx4 v2, s[20:23], s40 offen offset:0 lds\n\t"
      "v_mfma_f32_16x16x32_bf16 a[4:7], v[168:171], v[76:79], 0\n\t"
      "v_mfma_f32_16x16x32_bf16 a[36:39], v[168:171], v[80:83], 0\n\t"
      "v_mfma_f32_16x16x32_bf16 a[68:71], v[168:171], v[84:87], 0\n\t"
      "v_mfma_f32_16x16x32_bf16 v[48:51], v[168:171], v[88:91], 0\n\t"
      "s_waitcnt vmcnt(5)\n\t"
      "ds_read_b64_tr_b16 v[180:181], v6 offset:13312\n\t"
      "buffer_load_dwordx4 v3, s[20:23], s40 offen offset:1024 lds\n\t"
      "v_mfma_f32_16x16x32_bf16 a[20:23], v[168:171], v[92:95], 0\n\t"
      "ds_read_b64_tr_b16 v[182:183], v6 offset:13824\n\t"
      "v_mfma_f32_16x16x32_bf16 a[52:55], v[168:171], v[96:99], 0\n\t"
      "ds_read_b64_tr_b16 v[184:185], v7 offset:13312\n\t"
      "v_mfma_f32_16x16x32_bf16 a[84:87], v[168:171], v[100:103], 0\n\t"
      "ds_read_b64_tr_b16 v[186:187], v7 offset:13824\n\t"
      "v_mfma_f32_16x16x32_bf16 v[64:67], v[168:171], v[104:107], 0\n\t"
      "ds_read_b64_tr_b16 v[188:189], v8 offset:13312\n\t"
      "buffer_load_dwordx4 v4, s[20:23], s40 offen offset:2048 lds\n\t"
      "v_mfma_f32_16x16x32_bf16 a[8:11], v[172:175], v[76:79], 0\n\t"
      "ds_read_b64_tr_b16 v[190:191], v8 offset:13824\n\t"
      "v_mfma_f32_16x16x32_bf16 a[40:43], v[172:175], v[80:83], 0\n\t"
      "ds_read_b64_tr_b16 v[192:193], v9 offset:13312\n\t"
      "v_mfma_f32_16x16x32_bf16 a[72:75], v[172:175], v[84:87], 0\n\t"
      "ds_read_b64_tr_b16 v[194:195], v9 offset:13824\n\t"
      "v_mfma_f32_16x16x32_bf16 v[52:55], v[172:175], v[88:91], 0\n\t"
      "s_waitcnt vmcnt(3)\n\t"
      "ds_read_b64_tr_b16 v[196:197], v6 offset:17408\n\t"
      "buffer_load_dwordx4 v5, s[20:23], s40 offen offset:3072 lds\n\t"
      "v_mfma_f32_16x16x32_bf16 a[24:27], v[172:175], v[92:95], 0\n\t"
      "ds_read_b64_tr_b16 v[198:199], v6 offset:17920\n\t"
      "v_mfma_f32_16x16x32_bf16 a[56:59], v[172:175], v[96:99], 0\n\t"
      "ds_read_b64_tr_b16 v[200:201], v7 offset:17408\n\t"
      "v_mfma_f32_16x16x32_bf16 a[88:91], v[172:175], v[100:103], 0\n\t"
      "ds_read_b64_tr_b16 v[202:203], v7 offset:17920\n\t"
      "s_add_u32 m0, s37, 25600\n\t"
      "s_mov_b32 s40, 4194304\n\t"
      "v_mfma_f32_16x16x32_bf16 v[68:71], v[172:175], v[104:107], 0\n\t"
      "ds_read_b64_tr_b16 v[204:205], v8 offset:17408\n\t"
      "buffer_load_dwordx4 v2, s[20:23], s40 offen offset:0 lds\n\t"
      "v_mfma_f32_16x16x32_bf16 a[12:15], v[176:179], v[76:79], 0\n\t"
      "ds_read_b64_tr_b16 v[206:207], v8 offset:17920\n\t"
      "v_mfma_f32_16x16x32_bf16 a[44:47], v[176:179], v[80:83], 0\n\t"
      "ds_read_b64_tr_b16 v[208:209], v9 offset:17408\n\t"
      "v_mfma_f32_16x16x32_bf16 a[76:79], v[176:179], v[84:87], 0\n\t"
      "ds_read_b64_tr_b16 v[210:211], v9 offset:17920\n\t"
      "v_mfma_f32_16x16x32_bf16 v[56:59], v[176:179], v[88:91], 0\n\t"
      "buffer_load_dwordx4 v3, s[20:23], s40 offen offset:1024 lds\n\t"
      "v_mfma_f32_16x16x32_bf16 a[28:31], v[176:179], v[92:95], 0\n\t"
      "v_mfma_f32_16x16x32_bf16 a[60:63], v[176:179], v[96:99], 0\n\t"
      "v_mfma_f32_16x16x32_bf16 a[92:95], v[176:179], v[100:103], 0\n\t"
      "v_mfma_f32_16x16x32_bf16 v[72:75], v[176:179], v[104:107], 0\n\t"
      "buffer_load_dwordx4 v4, s[20:23], s40 offen offset:2048 lds\n\t"
      "s_waitcnt lgkmcnt(0)\n\t"
      "v_mfma_f32_16x16x32_bf16 a[0:3], v[180:183], v[80:83], a[0:3]\n\t"
      "v_mfma_f32_16x16x32_bf16 a[0:3], v[196:199], v[84:87], a[0:3]\n\t"
      "v_mfma_f32_16x16x32_bf16 a[32:35], v[180:183], v[84:87], a[32:35]\n\t"
      "v_mfma_f32_16x16x32_bf16 a[32:35], v[196:199], v[88:91], a[32:35]\n\t"
      "buffer_load_dwordx4 v5, s[20:23], s40 offen offset:3072 lds\n\t"
      "v_mfma_f32_16x16x32_bf16 a[64:67], v[180:183], v[88:91], a[64:67]\n\t"
      "v_mfma_f32_16x16x32_bf16 a[64:67], v[196:199], v[92:95], a[64:67]\n\t"
      "v_mfma_f32_16x16x32_bf16 v[44:47], v[180:183], v[92:95], v[44:47]\n\t"
      "s_add_u32 m0, s37, 29696\n\t"
      "s_mov_b32 s40, 5242880\n\t"
      "v_mfma_f32_16x16x32_bf16 v[44:47], v[196:199], v[96:99], v[44:47]\n\t"
      "buffer_load_dwordx4 v2, s[20:23], s40 offen offset:0 lds\n\t"
      "v_mfma_f32_16x16x32_bf16 a[16:19], v[180:183], v[96:99], a[16:19]\n\t"
      "v_mfma_f32_16x16x32_bf16 a[16:19], v[196:199], v[100:103], a[16:19]\n\t"
      "v_mfma_f32_16x16x32_bf16 a[48:51], v[180:183], v[100:103], a[48:51]\n\t"
      "v_mfma_f32_16x16x32_bf16 a[48:51], v[196:199], v[104:107], a[48:51]\n\t"
      "buffer_load_dwordx4 v3, s[20:23], s40 offen offset:1024 lds\n\t"
      "v_mfma_f32_16x16x32_bf16 a[80:83], v[180:183], v[104:107], a[80:83]\n\t"
      "v_mfma_f32_16x16x32_bf16 a[80:83], v[196:199], v[108:111], a[80:83]\n\t"
      "v_mfma_f32_16x16x32_bf16 v[60:63], v[180:183], v[108:111], v[60:63]\n\t"
      "v_mfma_f32_16x16x32_bf16 v[60:63], v[196:199], v[112:115], v[60:63]\n\t"
      "buffer_load_dwordx4 v4, s[20:23], s40 offen offset:2048 lds\n\t"
      "v_mfma_f32_16x16x32_bf16 a[4:7], v[184:187], v[80:83], a[4:7]\n\t"
      "v_mfma_f32_16x16x32_bf16 a[4:7], v[200:203], v[84:87], a[4:7]\n\t"
      "v_mfma_f32_16x16x32_bf16 a[36:39], v[184:187], v[84:87], a[36:39]\n\t"
      "v_mfma_f32_16x16x32_bf16 a[36:39], v[200:203], v[88:91], a[36:39]\n\t"
      "buffer_load_dwordx4 v5, s[20:23], s40 offen offset:3072 lds\n\t"
      "v_mfma_f32_16x16x32_bf16 a[68:71], v[184:187], v[88:91], a[68:71]\n\t"
      "v_mfma_f32_16x16x32_bf16 a[68:71], v[200:203], v[92:95], a[68:71]\n\t"
      "v_mfma_f32_16x16x32_bf16 v[48:51], v[184:187], v[92:95], v[48:51]\n\t"
      "s_add_u32 m0, s37, 33792\n\t"
      "s_mov_b32 s40, 6291456\n\t"
      "v_mfma_f32_16x16x32_bf16 v[48:51], v[200:203], v[96:99], v[48:51]\n\t"
      "buffer_load_dwordx4 v2, s[20:23], s40 offen offset:0 lds\n\t"
      "v_mfma_f32_16x16x32_bf16 a[20:23], v[184:187], v[96:99], a[20:23]\n\t"
      "v_mfma_f32_16x16x32_bf16 a[20:23], v[200:203], v[100:103], a[20:23]\n\t"
      "v_mfma_f32_16x16x32_bf16 a[52:55], v[184:187], v[100:103], a[52:55]\n\t"
      "v_mfma_f32_16x16x32_bf16 a[52:55], v[200:203], v[104:107], a[52:55]\n\t"
      "buffer_load_dwordx4 v3, s[20:23], s40 offen offset:1024 lds\n\t"
      "v_mfma_f32_16x16x32_bf16 a[84:87], v[184:187], v[104:107], a[84:87]\n\t"
      "v_mfma_f32_16x16x32_bf16 a[84:87], v[200:203], v[108:111], a[84:87]\n\t"
      "s_waitcnt vmcnt(10)\n\t"
      "ds_read_b64_tr_b16 v[212:213], v6 offset:21504\n\t"
      "v_mfma_f32_16x16x32_bf16 v[64:67], v[184:187], v[108:111], v[64:67]\n\t"
      "ds_read_b64_tr_b16 v[214:215], v6 offset:22016\n\t"
      "v_mfma_f32_16x16x32_bf16 v[64:67], v[200:203], v[112:115], v[64:67]\n\t"
      "ds_read_b64_tr_b16 v[216:217], v7 offset:21504\n\t"
      "buffer_load_dwordx4 v4, s[20:23], s40 offen offset:2048 lds\n\t"
      "v_mfma_f32_16x16x32_bf16 a[8:11], v[188:191], v[80:83], a[8:11]\n\t"
      "ds_read_b64_tr_b16 v[218:219], v7 offset:22016\n\t"
      "v_mfma_f32_16x16x32_bf16 a[8:11], v[204:207], v[84:87], a[8:11]\n\t"
      "ds_read_b64_tr_b16 v[220:221], v8 offset:21504\n\t"
      "v_mfma_f32_16x16x32_bf16 a[40:43], v[188:191], v[84:87], a[40:43]\n\t"
      "ds_read_b64_tr_b16 v[222:223], v8 offset:22016\n\t"
      "v_mfma_f32_16x16x32_bf16 a[40:43], v[204:207], v[88:91], a[40:43]\n\t"
      "ds_read_b64_tr_b16 v[224:225], v9 offset:21504\n\t"
      "buffer_load_dwordx4 v5, s[20:23], s40 offen offset:3072 lds\n\t"
      "v_mfma_f32_16x16x32_bf16 a[72:75], v[188:191], v[88:91], a[72:75]\n\t"
      "ds_read_b64_tr_b16 v[226:227], v9 offset:22016\n\t"
      "v_mfma_f32_16x16x32_bf16 a[72:75], v[204:207], v[92:95], a[72:75]\n\t"
      "v_mfma_f32_16x16x32_bf16 v[52:55], v[188:191], v[92:95], v[52:55]\n\t"
      "s_add_u32 m0, s37, 9216\n\t"
      "s_mov_b32 s40, 7340032\n\t"
      "v_mfma_f32_16x16x32_bf16 v[52:55], v[204:207], v[96:99], v[52:55]\n\t"
      "buffer_load_dwordx4 v2, s[20:23], s40 offen offset:0 lds\n\t"
      "v_mfma_f32_16x16x32_bf16 a[24:27], v[188:191], v[96:99], a[24:27]\n\t"
      "v_mfma_f32_16x16x32_bf16 a[24:27], v[204:207], v[100:103], a[24:27]\n\t"
      "v_mfma_f32_16x16x32_bf16 a[56:59], v[188:191], v[100:103], a[56:59]\n\t"
      "v_mfma_f32_16x16x32_bf16 a[56:59], v[204:207], v[104:107], a[56:59]\n\t"
      "buffer_load_dwordx4 v3, s[20:23], s40 offen offset:1024 lds\n\t"
      "v_mfma_f32_16x16x32_bf16 a[88:91], v[188:191], v[104:107], a[88:91]\n\t"
      "v_mfma_f32_16x16x32_bf16 a[88:91], v[204:207], v[108:111], a[88:91]\n\t"
      "v_mfma_f32_16x16x32_bf16 v[68:71], v[188:191], v[108:111], v[68:71]\n\t"
      "v_mfma_f32_16x16x32_bf16 v[68:71], v[204:207], v[112:115], v[68:71]\n\t"
      "buffer_load_dwordx4 v4, s[20:23], s40 offen offset:2048 lds\n\t"
      "v_mfma_f32_16x16x32_bf16 a[12:15], v[192:195], v[80:83], a[12:15]\n\t"
      "v_mfma_f32_16x16x32_bf16 a[12:15], v[208:211], v[84:87], a[12:15]\n\t"
      "v_mfma_f32_16x16x32_bf16 a[44:47], v[192:195], v[84:87], a[44:47]\n\t"
      "v_mfma_f32_16x16x32_bf16 a[44:47], v[208:211], v[88:91], a[44:47]\n\t"
      "s_waitcnt vmcnt(11)\n\t"
      "ds_read_b64_tr_b16 v[228:229], v6 offset:25600\n\t"
      "buffer_load_dwordx4 v5, s[20:23], s40 offen offset:3072 lds\n\t"
      "v_mfma_f32_16x16x32_bf16 a[76:79], v[192:195], v[88:91], a[76:79]\n\t"
      "ds_read_b64_tr_b16 v[230:231], v6 offset:26112\n\t"
      "v_mfma_f32_16x16x32_bf16 a[76:79], v[208:211], v[92:95], a[76:79]\n\t"
      "ds_read_b64_tr_b16 v[232:233], v7 offset:25600\n\t"
      "v_mfma_f32_16x16x32_bf16 v[56:59], v[192:195], v[92:95], v[56:59]\n\t"
      "ds_read_b64_tr_b16 v[234:235], v7 offset:26112\n\t"
      "s_add_u32 m0, s37, 13312\n\t"
      "s_mov_b32 s40, 8388608\n\t"
      "v_mfma_f32_16x16x32_bf16 v[56:59], v[208:211], v[96:99], v[56:59]\n\t"
      "ds_read_b64_tr_b16 v[236:237], v8 offset:25600\n\t"
      "buffer_load_dwordx4 v2, s[20:23], s40 offen offset:0 lds\n\t"
      "v_mfma_f32_16x16x32_bf16 a[28:31], v[192:195], v[96:99], a[28:31]\n\t"
      "ds_read_b64_tr_b16 v[238:239], v8 offset:26112\n\t"
      "v_mfma_f32_16x16x32_bf16 a[28:31], v[208:211], v[100:103], a[28:31]\n\t"
      "ds_read_b64_tr_b16 v[240:241], v9 offset:25600\n\t"
      "v_mfma_f32_16x16x32_bf16 a[60:63], v[192:195], v[100:103], a[60:63]\n\t"
      "ds_read_b64_tr_b16 v[242:243], v9 offset:26112\n\t"
      "v_mfma_f32_16x16x32_bf16 a[60:63], v[208:211], v[104:107], a[60:63]\n\t"
      "buffer_load_dwordx4 v3, s[20:23], s40 offen offset:1024 lds\n\t"
      "v_mfma_f32_16x16x32_bf16 a[92:95], v[192:195], v[104:107], a[92:95]\n\t"
      "v_mfma_f32_16x16x32_bf16 a[92:95], v[208:211], v[108:111], a[92:95]\n\t"
      "v_mfma_f32_16x16x32_bf16 v[72:75], v[192:195], v[108:111], v[72:75]\n\t"
      "v_mfma_f32_16x16x32_bf16 v[72:75], v[208:211], v[112:115], v[72:75]\n\t"
      "buffer_load_dwordx4 v4, s[20:23], s40 offen offset:2048 lds\n\t"
      "s_waitcnt lgkmcnt(0)\n\t"
      "v_mfma_f32_16x16x32_bf16 a[0:3], v[212:215], v[88:91], a[0:3]\n\t"
      "v_mfma_f32_16x16x32_bf16 a[0:3], v[228:231], v[92:95], a[0:3]\n\t"
      "v_mfma_f32_16x16x32_bf16 a[32:35], v[212:215], v[92:95], a[32:35]\n\t"
      "v_mfma_f32_16x16x32_bf16 a[32:35], v[228:231], v[96:99], a[32:35]\n\t"
      "buffer_load_dwordx4 v5, s[20:23], s40 offen offset:3072 lds\n\t"
      "v_mfma_f32_16x16x32_bf16 a[64:67], v[212:215], v[96:99], a[64:67]\n\t"
      "v_mfma_f32_16x16x32_bf16 a[64:67], v[228:231], v[100:103], a[64:67]\n\t"
      "v_mfma_f32_16x16x32_bf16 v[44:47], v[212:215], v[100:103], v[44:47]\n\t"
      "s_add_u32 m0, s37, 17408\n\t"
      "s_mov_b32 s40, 9437184\n\t"
      "v_mfma_f32_16x16x32_bf16 v[44:47], v[228:231], v[104:107], v[44:47]\n\t"
      "buffer_load_dwordx4 v2, s[20:23], s40 offen offset:0 lds\n\t"
      "v_mfma_f32_16x16x32_bf16 a[16:19], v[212:215], v[104:107], a[16:19]\n\t"
      "v_mfma_f32_16x16x32_bf16 a[16:19], v[228:231], v[108:111], a[16:19]\n\t"
      "v_mfma_f32_16x16x32_bf16 a[48:51], v[212:215], v[108:111], a[48:51]\n\t"
      "v_mfma_f32_16x16x32_bf16 a[48:51], v[228:231], v[112:115], a[48:51]\n\t"
      "buffer_load_dwordx4 v3, s[20:23], s40 offen offset:1024 lds\n\t"
      "v_mfma_f32_16x16x32_bf16 a[80:83], v[212:215], v[112:115], a[80:83]\n\t"
      "v_mfma_f32_16x16x32_bf16 a[80:83], v[228:231], v[116:119], a[80:83]\n\t"
      "v_mfma_f32_16x16x32_bf16 v[60:63], v[212:215], v[116:119], v[60:63]\n\t"
      "v_mfma_f32_16x16x32_bf16 v[60:63], v[228:231], v[120:123], v[60:63]\n\t"
      "buffer_load_dwordx4 v4, s[20:23], s40 offen offset:2048 lds\n\t"
      "v_mfma_f32_16x16x32_bf16 a[4:7], v[216:219], v[88:91], a[4:7]\n\t"
      "v_mfma_f32_16x16x32_bf16 a[4:7], v[232:235], v[92:95], a[4:7]\n\t"
      "v_mfma_f32_16x16x32_bf16 a[36:39], v[216:219], v[92:95], a[36:39]\n\t"
      "v_mfma_f32_16x16x32_bf16 a[36:39], v[232:235], v[96:99], a[36:39]\n\t"
      "buffer_load_dwordx4 v5, s[20:23], s40 offen offset:3072 lds\n\t"
      "v_mfma_f32_16x16x32_bf16 a[68:71], v[216:219], v[96:99], a[68:71]\n\t"
      "v_mfma_f32_16x16x32_bf16 a[68:71], v[232:235], v[100:103], a[68:71]\n\t"
      "v_mfma_f32_16x16x32_bf16 v[48:51], v[216:219], v[100:103], v[48:51]\n\t"
      "s_add_u32 m0, s37, 21504\n\t"
      "s_mov_b32 s40, 10485760\n\t"
      "v_mfma_f32_16x16x32_bf16 v[48:51], v[232:235], v[104:107], v[48:51]\n\t"
      "buffer_load_dwordx4 v2, s[20:23], s40 offen offset:0 lds\n\t"
      "v_mfma_f32_16x16x32_bf16 a[20:23], v[216:219], v[104:107], a[20:23]\n\t"
      "v_mfma_f32_16x16x32_bf16 a[20:23], v[232:235], v[108:111], a[20:23]\n\t"
      "v_mfma_f32_16x16x32_bf16 a[52:55], v[216:219], v[108:111], a[52:55]\n\t"
      "v_mfma_f32_16x16x32_bf16 a[52:55], v[232:235], v[112:115], a[52:55]\n\t"
      "buffer_load_dwordx4 v3, s[20:23], s40 offen offset:1024 lds\n\t"
      "v_mfma_f32_16x16x32_bf16 a[84:87], v[216:219], v[112:115], a[84:87]\n\t"
      "v_mfma_f32_16x16x32_bf16 a[84:87], v[232:235], v[116:119], a[84:87]\n\t"
      "s_waitcnt vmcnt(18)\n\t"
      "ds_read_b64_tr_b16 v[244:245], v6 offset:29696\n\t"
      "v_mfma_f32_16x16x32_bf16 v[64:67], v[216:219], v[116:119], v[64:67]\n\t"
      "ds_read_b64_tr_b16 v[246:247], v6 offset:30208\n\t"
      "v_mfma_f32_16x16x32_bf16 v[64:67], v[232:235], v[120:123], v[64:67]\n\t"
      "ds_read_b64_tr_b16 v[248:249], v7 offset:29696\n\t"
      "buffer_load_dwordx4 v4, s[20:23], s40 offen offset:2048 lds\n\t"
      "v_mfma_f32_16x16x32_bf16 a[8:11], v[220:223], v[88:91], a[8:11]\n\t"
      "ds_read_b64_tr_b16 v[250:251], v7 offset:30208\n\t"
      "v_mfma_f32_16x16x32_bf16 a[8:11], v[236:239], v[92:95], a[8:11]\n\t"
      "ds_read_b64_tr_b16 v[252:253], v8 offset:29696\n\t"
      "v_mfma_f32_16x16x32_bf16 a[40:43], v[220:223], v[92:95], a[40:43]\n\t"
      "ds_read_b64_tr_b16 v[254:255], v8 offset:30208\n\t"
      "v_mfma_f32_16x16x32_bf16 a[40:43], v[236:239], v[96:99], a[40:43]\n\t"
      "ds_read_b64_tr_b16 v[164:165], v9 offset:29696\n\t"
      "buffer_load_dwordx4 v5, s[20:23], s40 offen offset:3072 lds\n\t"
      "v_mfma_f32_16x16x32_bf16 a[72:75], v[220:223], v[96:99], a[72:75]\n\t"
      "ds_read_b64_tr_b16 v[166:167], v9 offset:30208\n\t"
      "v_mfma_f32_16x16x32_bf16 a[72:75], v[236:239], v[100:103], a[72:75]\n\t"
      "v_mfma_f32_16x16x32_bf16 v[52:55], v[220:223], v[100:103], v[52:55]\n\t"
      "s_add_u32 m0, s37, 25600\n\t"
      "s_mov_b32 s40, 11534336\n\t"
      "v_mfma_f32_16x16x32_bf16 v[52:55], v[236:239], v[104:107], v[52:55]\n\t"
      "buffer_load_dwordx4 v2, s[20:23], s40 offen offset:0 lds\n\t"
      "v_mfma_f32_16x16x32_bf16 a[24:27], v[220:223], v[104:107], a[24:27]\n\t"
      "v_mfma_f32_16x16x32_bf16 a[24:27], v[236:239], v[108:111], a[24:27]\n\t"
      "v_mfma_f32_16x16x32_bf16 a[56:59], v[220:223], v[108:111], a[56:59]\n\t"
      "v_mfma_f32_16x16x32_bf16 a[56:59], v[236:239], v[112:115], a[56:59]\n\t"
      "buffer_load_dwordx4 v3, s[20:23], s40 offen offset:1024 lds\n\t"
      "v_mfma_f32_16x16x32_bf16 a[88:91], v[220:223], v[112:115], a[88:91]\n\t"
      "v_mfma_f32_16x16x32_bf16 a[88:91], v[236:239], v[116:119], a[88:91]\n\t"
      "v_mfma_f32_16x16x32_bf16 v[68:71], v[220:223], v[116:119], v[68:71]\n\t"
      "v_mfma_f32_16x16x32_bf16 v[68:71], v[236:239], v[120:123], v[68:71]\n\t"
      "buffer_load_dwordx4 v4, s[20:23], s40 offen offset:2048 lds\n\t"
      "v_mfma_f32_16x16x32_bf16 a[12:15], v[224:227], v[88:91], a[12:15]\n\t"
      "v_mfma_f32_16x16x32_bf16 a[12:15], v[240:243], v[92:95], a[12:15]\n\t"
      "v_mfma_f32_16x16x32_bf16 a[44:47], v[224:227], v[92:95], a[44:47]\n\t"
      "v_mfma_f32_16x16x32_bf16 a[44:47], v[240:243], v[96:99], a[44:47]\n\t"
      "s_waitcnt vmcnt(19)\n\t"
      "ds_read_b64_tr_b16 v[168:169], v6 offset:33792\n\t"
      "buffer_load_dwordx4 v5, s[20:23], s40 offen offset:3072 lds\n\t"
      "v_mfma_f32_16x16x32_bf16 a[76:79], v[224:227], v[96:99], a[76:79]\n\t"
      "ds_read_b64_tr_b16 v[170:171], v6 offset:34304\n\t"
      "v_mfma_f32_16x16x32_bf16 a[76:79], v[240:243], v[100:103], a[76:79]\n\t"
      "ds_read_b64_tr_b16 v[172:173], v7 offset:33792\n\t"
      "v_mfma_f32_16x16x32_bf16 v[56:59], v[224:227], v[100:103], v[56:59]\n\t"
      "ds_read_b64_tr_b16 v[174:175], v7 offset:34304\n\t"
      "v_mfma_f32_16x16x32_bf16 v[56:59], v[240:243], v[104:107], v[56:59]\n\t"
      "ds_read_b64_tr_b16 v[176:177], v8 offset:33792\n\t"
      "v_mfma_f32_16x16x32_bf16 a[28:31], v[224:227], v[104:107], a[28:31]\n\t"
      "ds_read_b64_tr_b16 v[178:179], v8 offset:34304\n\t"
      "s_barrier\n\t"
      "v_mfma_f32_16x16x32_bf16 a[28:31], v[240:243], v[108:111], a[28:31]\n\t"
      "ds_read_b64_tr_b16 v[180:181], v9 offset:33792\n\t"
      "v_mfma_f32_16x16x32_bf16 a[60:63], v[224:227], v[108:111], a[60:63]\n\t"
      "ds_read_b64_tr_b16 v[182:183], v9 offset:34304\n\t"
      "v_mfma_f32_16x16x32_bf16 a[60:63], v[240:243], v[112:115], a[60:63]\n\t"
      "s_mov_b32 s57, 12582912\n\t"
      "buffer_load_dword v35, v33, s[20:23], s57 offen\n\t"
      "v_mfma_f32_16x16x32_bf16 a[92:95], v[224:227], v[112:115], a[92:95]\n\t"
      "s_mov_b32 s57, 262144\n\t"
      "s_mov_b32 exec_lo, 0xfffffff\n\t"
      "s_mov_b32 exec_hi, 0x0\n\t"
      "buffer_load_dword v35, v33, s[20:23], s57 offen\n\t"
      "s_mov_b64 exec, -1\n\t"
      "v_mfma_f32_16x16x32_bf16 a[92:95], v[240:243], v[116:119], a[92:95]\n\t"
      "s_and_b32 s33, s33, 0xffff\n\t"
      "s_mov_b32 s34, Lce%=-Lcs%=\n\t"
      "s_mov_b32 s35, 0x00020000\n\t"
      "v_and_b32 v42, 0xc1, %2\n\t"
      "v_mul_u32_u24 v42, 0x84, v42\n\t"
      "s_lshl_b32 s41, %1, 7\n\t"
      "v_and_b32 v42, 0x380, v42\n\t"
      "s_and_b32 s41, s41, 0x7c00\n\t"
      "v_or_b32 v42, s41, v42\n\t"
      "s_mov_b64 exec, 3\n\t"
      "buffer_load_dword v35, v42, s[32:35], 0 offen\n\t"
      "s_mov_b64 exec, -1\n\t"
      "v_mfma_f32_16x16x32_bf16 v[72:75], v[224:227], v[116:119], v[72:75]\n\t"
      "s_mov_b32 m0, s59\n\t"
      "v_mfma_f32_16x16x32_bf16 v[72:75], v[240:243], v[120:123], v[72:75]\n\t"
      "buffer_load_dwordx4 v22, s[16:19], s52 offen offset:0 lds\n\t"
      "s_waitcnt lgkmcnt(0)\n\t"
      "v_mfma_f32_16x16x32_bf16 a[0:3], v[244:247], v[96:99], a[0:3]\n\t"
      "v_mfma_f32_16x16x32_bf16 a[0:3], v[168:171], v[100:103], a[0:3]\n\t"
      "v_mfma_f32_16x16x32_bf16 a[32:35], v[244:247], v[100:103], a[32:35]\n\t"
      "s_mov_b32 m0, s100\n\t"
      "v_mfma_f32_16x16x32_bf16 a[32:35], v[168:171], v[104:107], a[32:35]\n\t"
      "buffer_load_dwordx4 v23, s[16:19], s52 offen offset:0 lds\n\t"
      "v_mfma_f32_16x16x32_bf16 a[64:67], v[244:247], v[104:107], a[64:67]\n\t"
      "v_mfma_f32_16x16x32_bf16 a[64:67], v[168:171], v[108:111], a[64:67]\n\t"
      "v_mfma_f32_16x16x32_bf16 v[44:47], v[244:247], v[108:111], v[44:47]\n\t"
      "v_mfma_f32_16x16x32_bf16 v[44:47], v[168:171], v[112:115], v[44:47]\n\t"
      "buffer_load_dwordx4 v24, s[16:19], s52 offen offset:1024 lds\n\t"
      "v_mfma_f32_16x16x32_bf16 a[16:19], v[244:247], v[112:115], a[16:19]\n\t"
      "v_mfma_f32_16x16x32_bf16 a[16:19], v[168:171], v[116:119], a[16:19]\n\t"
      "v_mfma_f32_16x16x32_bf16 a[48:51], v[244:247], v[116:119], a[48:51]\n\t"
      "v_mfma_f32_16x16x32_bf16 a[48:51], v[168:171], v[120:123], a[48:51]\n\t"
      "buffer_load_dwordx4 v25, s[16:19], s52 offen offset:2048 lds\n\t"
      "v_mfma_f32_16x16x32_bf16 a[80:83], v[244:247], v[120:123], a[80:83]\n\t"
      "v_mfma_f32_16x16x32_bf16 a[80:83], v[168:171], v[124:127], a[80:83]\n\t"
      "v_mfma_f32_16x16x32_bf16 v[60:63], v[244:247], v[124:127], v[60:63]\n\t"
      "s_add_u32 m0, s37, 8192\n\t"
      "v_mfma_f32_16x16x32_bf16 v[60:63], v[168:171], v[128:131], v[60:63]\n\t"
      "buffer_load_dwordx4 v26, s[16:19], s52 offen offset:0 lds\n\t"
      "v_mfma_f32_16x16x32_bf16 a[4:7], v[248:251], v[96:99], a[4:7]\n\t"
      "v_mfma_f32_16x16x32_bf16 a[4:7], v[172:175], v[100:103], a[4:7]\n\t"
      "v_mfma_f32_16x16x32_bf16 a[36:39], v[248:251], v[100:103], a[36:39]\n\t"
      "v_mfma_f32_16x16x32_bf16 a[36:39], v[172:175], v[104:107], a[36:39]\n\t"
      "v_mfma_f32_16x16x32_bf16 a[68:71], v[248:251], v[104:107], a[68:71]\n\t"
      "v_mfma_f32_16x16x32_bf16 a[68:71], v[172:175], v[108:111], a[68:71]\n\t"
      "v_mfma_f32_16x16x32_bf16 v[48:51], v[248:251], v[108:111], v[48:51]\n\t"
      "v_mfma_f32_16x16x32_bf16 v[48:51], v[172:175], v[112:115], v[48:51]\n\t"
      "v_mfma_f32_16x16x32_bf16 a[20:23], v[248:251], v[112:115], a[20:23]\n\t"
      "v_mfma_f32_16x16x32_bf16 a[20:23], v[172:175], v[116:119], a[20:23]\n\t"
      "v_mfma_f32_16x16x32_bf16 a[52:55], v[248:251], v[116:119], a[52:55]\n\t"
      "v_mfma_f32_16x16x32_bf16 a[52:55], v[172:175], v[120:123], a[52:55]\n\t"
      "v_mfma_f32_16x16x32_bf16 a[84:87], v[248:251], v[120:123], a[84:87]\n\t"
      "v_mfma_f32_16x16x32_bf16 a[84:87], v[172:175], v[124:127], a[84:87]\n\t"
      "s_waitcnt vmcnt(24)\n\t"
      "ds_read_b64_tr_b16 v[196:197], v6 offset:9216\n\t"
      "v_mfma_f32_16x16x32_bf16 v[64:67], v[248:251], v[124:127], v[64:67]\n\t"
      "ds_read_b64_tr_b16 v[198:199], v6 offset:9728\n\t"
      "v_mfma_f32_16x16x32_bf16 v[64:67], v[172:175], v[128:131], v[64:67]\n\t"
      "ds_read_b64_tr_b16 v[184:185], v7 offset:9216\n\t"
      "v_mfma_f32_16x16x32_bf16 a[8:11], v[252:255], v[96:99], a[8:11]\n\t"
      "ds_read_b64_tr_b16 v[186:187], v7 offset:9728\n\t"
      "v_mfma_f32_16x16x32_bf16 a[8:11], v[176:179], v[100:103], a[8:11]\n\t"
      "ds_read_b64_tr_b16 v[200:201], v8 offset:9216\n\t"
      "v_mfma_f32_16x16x32_bf16 a[40:43], v[252:255], v[100:103], a[40:43]\n\t"
      "ds_read_b64_tr_b16 v[202:203], v8 offset:9728\n\t"
      "v_mfma_f32_16x16x32_bf16 a[40:43], v[176:179], v[104:107], a[40:43]\n\t"
      "ds_read_b64_tr_b16 v[188:189], v9 offset:9216\n\t"
      "v_mfma_f32_16x16x32_bf16 a[72:75], v[252:255], v[104:107], a[72:75]\n\t"
      "ds_read_b64_tr_b16 v[190:191], v9 offset:9728\n\t"
      "v_mfma_f32_16x16x32_bf16 a[72:75], v[176:179], v[108:111], a[72:75]\n\t"
      "v_mfma_f32_16x16x32_bf16 v[52:55], v[252:255], v[108:111], v[52:55]\n\t"
      "v_mfma_f32_16x16x32_bf16 v[52:55], v[176:179], v[112:115], v[52:55]\n\t"
      "v_mfma_f32_16x16x32_bf16 a[24:27], v[252:255], v[112:115], a[24:27]\n\t"
      "v_mfma_f32_16x16x32_bf16 a[24:27], v[176:179], v[116:119], a[24:27]\n\t"
      "v_mfma_f32_16x16x32_bf16 a[56:59], v[252:255], v[116:119], a[56:59]\n\t"
      "v_mfma_f32_16x16x32_bf16 a[56:59], v[176:179], v[120:123], a[56:59]\n\t"
      "v_mfma_f32_16x16x32_bf16 a[88:91], v[252:255], v[120:123], a[88:91]\n\t"
      "v_mfma_f32_16x16x32_bf16 a[88:91], v[176:179], v[124:127], a[88:91]\n\t"
      "v_mfma_f32_16x16x32_bf16 v[68:71], v[252:255], v[124:127], v[68:71]\n\t"
      "v_mfma_f32_16x16x32_bf16 v[68:71], v[176:179], v[128:131], v[68:71]\n\t"
      "v_mfma_f32_16x16x32_bf16 a[12:15], v[164:167], v[96:99], a[12:15]\n\t"
      "v_mfma_f32_16x16x32_bf16 a[12:15], v[180:183], v[100:103], a[12:15]\n\t"
      "v_mfma_f32_16x16x32_bf16 a[44:47], v[164:167], v[100:103], a[44:47]\n\t"
      "v_mfma_f32_16x16x32_bf16 a[44:47], v[180:183], v[104:107], a[44:47]\n\t"
      "s_waitcnt vmcnt(20)\n\t"
      "ds_read_b64_tr_b16 v[204:205], v6 offset:13312\n\t"
      "v_mfma_f32_16x16x32_bf16 a[76:79], v[164:167], v[104:107], a[76:79]\n\t"
      "ds_read_b64_tr_b16 v[206:207], v6 offset:13824\n\t"
      "v_mfma_f32_16x16x32_bf16 a[76:79], v[180:183], v[108:111], a[76:79]\n\t"
      "ds_read_b64_tr_b16 v[192:193], v7 offset:13312\n\t"
      "v_mfma_f32_16x16x32_bf16 v[56:59], v[164:167], v[108:111], v[56:59]\n\t"
      "ds_read_b64_tr_b16 v[194:195], v7 offset:13824\n\t"
      "v_mfma_f32_16x16x32_bf16 v[56:59], v[180:183], v[112:115], v[56:59]\n\t"
      "ds_read_b64_tr_b16 v[208:209], v8 offset:13312\n\t"
      "v_mfma_f32_16x16x32_bf16 a[28:31], v[164:167], v[112:115], a[28:31]\n\t"
      "ds_read_b64_tr_b16 v[210:211], v8 offset:13824\n\t"
      "v_mfma_f32_16x16x32_bf16 a[28:31], v[180:183], v[116:119], a[28:31]\n\t"
      "ds_read_b64_tr_b16 v[212:213], v9 offset:13312\n\t"
      "v_mfma_f32_16x16x32_bf16 a[60:63], v[164:167], v[116:119], a[60:63]\n\t"
      "ds_read_b64_tr_b16 v[214:215], v9 offset:13824\n\t"
      "v_mfma_f32_16x16x32_bf16 a[60:63], v[180:183], v[120:123], a[60:63]\n\t"
      "v_mfma_f32_16x16x32_bf16 a[92:95], v[164:167], v[120:123], a[92:95]\n\t"
      "v_mfma_f32_16x16x32_bf16 a[92:95], v[180:183], v[124:127], a[92:95]\n\t"
      "v_mfma_f32_16x16x32_bf16 v[72:75], v[164:167], v[124:127], v[72:75]\n\t"
      "v_mfma_f32_16x16x32_bf16 v[72:75], v[180:183], v[128:131], v[72:75]\n\t"
      "s_waitcnt lgkmcnt(0)\n\t"
      "v_mfma_f32_16x16x32_bf16 a[0:3], v[196:199], v[104:107], a[0:3]\n\t"
      "v_mfma_f32_16x16x32_bf16 a[0:3], v[204:207], v[108:111], a[0:3]\n\t"
      "v_mfma_f32_16x16x32_bf16 a[32:35], v[196:199], v[108:111], a[32:35]\n\t"
      "v_mfma_f32_16x16x32_bf16 a[32:35], v[204:207], v[112:115], a[32:35]\n\t"
      "v_mfma_f32_16x16x32_bf16 a[64:67], v[196:199], v[112:115], a[64:67]\n\t"
      "v_mfma_f32_16x16x32_bf16 a[64:67], v[204:207], v[116:119], a[64:67]\n\t"
      "v_mfma_f32_16x16x32_bf16 v[44:47], v[196:199], v[116:119], v[44:47]\n\t"
      "v_mfma_f32_16x16x32_bf16 v[44:47], v[204:207], v[120:123], v[44:47]\n\t"
      "v_mfma_f32_16x16x32_bf16 a[16:19], v[196:199], v[120:123], a[16:19]\n\t"
      "v_mfma_f32_16x16x32_bf16 a[16:19], v[204:207], v[124:127], a[16:19]\n\t"
      "v_mfma_f32_16x16x32_bf16 a[48:51], v[196:199], v[124:127], a[48:51]\n\t"
      "v_mfma_f32_16x16x32_bf16 a[48:51], v[204:207], v[128:131], a[48:51]\n\t"
      "v_mfma_f32_16x16x32_bf16 a[80:83], v[196:199], v[128:131], a[80:83]\n\t"
      "v_mfma_f32_16x16x32_bf16 a[80:83], v[204:207], v[132:135], a[80:83]\n\t"
      "v_mfma_f32_16x16x32_bf16 v[60:63], v[196:199], v[132:135], v[60:63]\n\t"
      "v_mfma_f32_16x16x32_bf16 v[60:63], v[204:207], v[136:139], v[60:63]\n\t"
      "v_mfma_f32_16x16x32_bf16 a[4:7], v[184:187], v[104:107], a[4:7]\n\t"
      "v_mfma_f32_16x16x32_bf16 a[4:7], v[192:195], v[108:111], a[4:7]\n\t"
      "v_mfma_f32_16x16x32_bf16 a[36:39], v[184:187], v[108:111], a[36:39]\n\t"
      "v_mfma_f32_16x16x32_bf16 a[36:39], v[192:195], v[112:115], a[36:39]\n\t"
      "v_mfma_f32_16x16x32_bf16 a[68:71], v[184:187], v[112:115], a[68:71]\n\t"
      "v_mfma_f32_16x16x32_bf16 a[68:71], v[192:195], v[116:119], a[68:71]\n\t"
      "v_mfma_f32_16x16x32_bf16 v[48:51], v[184:187], v[116:119], v[48:51]\n\t"
      "v_mfma_f32_16x16x32_bf16 v[48:51], v[192:195], v[120:123], v[48:51]\n\t"
      "v_mfma_f32_16x16x32_bf16 a[20:23], v[184:187], v[120:123], a[20:23]\n\t"
      "v_mfma_f32_16x16x32_bf16 a[20:23], v[192:195], v[124:127], a[20:23]\n\t"
      "v_mfma_f32_16x16x32_bf16 a[52:55], v[184:187], v[124:127], a[52:55]\n\t"
      "v_mfma_f32_16x16x32_bf16 a[52:55], v[192:195], v[128:131], a[52:55]\n\t"
      "s_add_u32 m0, s37, 29696\n\t"
      "s_mov_b32 s40, 12582912\n\t"
      "v_mfma_f32_16x16x32_bf16 a[84:87], v[184:187], v[128:131], a[84:87]\n\t"
      "buffer_load_dwordx4 v2, s[20:23], s40 offen offset:0 lds\n\t"
      "v_mfma_f32_16x16x32_bf16 a[84:87], v[192:195], v[132:135], a[84:87]\n\t"
      "s_waitcnt vmcnt(17)\n\t"
      "ds_read_b64_tr_b16 v[228:229], v6 offset:17408\n\t"
      "v_mfma_f32_16x16x32_bf16 v[64:67], v[184:187], v[132:135], v[64:67]\n\t"
      "ds_read_b64_tr_b16 v[230:231], v6 offset:17920\n\t"
      "v_mfma_f32_16x16x32_bf16 v[64:67], v[192:195], v[136:139], v[64:67]\n\t"
      "ds_read_b64_tr_b16 v[216:217], v7 offset:17408\n\t"
      "v_mfma_f32_16x16x32_bf16 a[8:11], v[200:203], v[104:107], a[8:11]\n\t"
      "ds_read_b64_tr_b16 v[218:219], v7 offset:17920\n\t"
      "buffer_load_dwordx4 v3, s[20:23], s40 offen offset:1024 lds\n\t"
      "v_mfma_f32_16x16x32_bf16 a[8:11], v[208:211], v[108:111], a[8:11]\n\t"
      "ds_read_b64_tr_b16 v[232:233], v8 offset:17408\n\t"
      "v_mfma_f32_16x16x32_bf16 a[40:43], v[200:203], v[108:111], a[40:43]\n\t"
      "ds_read_b64_tr_b16 v[234:235], v8 offset:17920\n\t"
      "v_mfma_f32_16x16x32_bf16 a[40:43], v[208:211], v[112:115], a[40:43]\n\t"
      "ds_read_b64_tr_b16 v[220:221], v9 offset:17408\n\t"
      "v_mfma_f32_16x16x32_bf16 a[72:75], v[200:203], v[112:115], a[72:75]\n\t"
      "ds_read_b64_tr_b16 v[222:223], v9 offset:17920\n\t"
      "buffer_load_dwordx4 v4, s[20:23], s40 offen offset:2048 lds\n\t"
      "v_mfma_f32_16x16x32_bf16 a[72:75], v[208:211], v[116:119], a[72:75]\n\t"
      "v_mfma_f32_16x16x32_bf16 v[52:55], v[200:203], v[116:119], v[52:55]\n\t"
      "v_mfma_f32_16x16x32_bf16 v[52:55], v[208:211], v[120:123], v[52:55]\n\t"
      "v_mfma_f32_16x16x32_bf16 a[24:27], v[200:203], v[120:123], a[24:27]\n\t"
      "buffer_load_dwordx4 v5, s[20:23], s40 offen offset:3072 lds\n\t"
      "v_mfma_f32_16x16x32_bf16 a[24:27], v[208:211], v[124:127], a[24:27]\n\t"
      "v_mfma_f32_16x16x32_bf16 a[56:59], v[200:203], v[124:127], a[56:59]\n\t"
      "v_mfma_f32_16x16x32_bf16 a[56:59], v[208:211], v[128:131], a[56:59]\n\t"
      "s_add_u32 m0, s37, 33792\n\t"
      "s_mov_b32 s40, 13631488\n\t"
      "v_mfma_f32_16x16x32_bf16 a[88:91], v[200:203], v[128:131], a[88:91]\n\t"
      "buffer_load_dwordx4 v2, s[20:23], s40 offen offset:0 lds\n\t"
      "v_mfma_f32_16x16x32_bf16 a[88:91], v[208:211], v[132:135], a[88:91]\n\t"
      "v_mfma_f32_16x16x32_bf16 v[68:71], v[200:203], v[132:135], v[68:71]\n\t"
      "v_mfma_f32_16x16x32_bf16 v[68:71], v[208:211], v[136:139], v[68:71]\n\t"
      "v_mfma_f32_16x16x32_bf16 a[12:15], v[188:191], v[104:107], a[12:15]\n\t"
      "buffer_load_dwordx4 v3, s[20:23], s40 offen offset:1024 lds\n\t"
      "v_mfma_f32_16x16x32_bf16 a[12:15], v[212:215], v[108:111], a[12:15]\n\t"
      "v_mfma_f32_16x16x32_bf16 a[44:47], v[188:191], v[108:111], a[44:47]\n\t"
      "v_mfma_f32_16x16x32_bf16 a[44:47], v[212:215], v[112:115], a[44:47]\n\t"
      "s_waitcnt vmcnt(18)\n\t"
      "ds_read_b64_tr_b16 v[236:237], v6 offset:21504\n\t"
      "v_mfma_f32_16x16x32_bf16 a[76:79], v[188:191], v[112:115], a[76:79]\n\t"
      "ds_read_b64_tr_b16 v[238:239], v6 offset:22016\n\t"
      "buffer_load_dwordx4 v4, s[20:23], s40 offen offset:2048 lds\n\t"
      "v_mfma_f32_16x16x32_bf16 a[76:79], v[212:215], v[116:119], a[76:79]\n\t"
      "ds_read_b64_tr_b16 v[224:225], v7 offset:21504\n\t"
      "v_mfma_f32_16x16x32_bf16 v[56:59], v[188:191], v[116:119], v[56:59]\n\t"
      "ds_read_b64_tr_b16 v[226:227], v7 offset:22016\n\t"
      "v_mfma_f32_16x16x32_bf16 v[56:59], v[212:215], v[120:123], v[56:59]\n\t"
      "ds_read_b64_tr_b16 v[240:241], v8 offset:21504\n\t"
      "v_mfma_f32_16x16x32_bf16 a[28:31], v[188:191], v[120:123], a[28:31]\n\t"
      "ds_read_b64_tr_b16 v[242:243], v8 offset:22016\n\t"
      "buffer_load_dwordx4 v5, s[20:23], s40 offen offset:3072 lds\n\t"
      "v_mfma_f32_16x16x32_bf16 a[28:31], v[212:215], v[124:127], a[28:31]\n\t"
      "ds_read_b64_tr_b16 v[244:245], v9 offset:21504\n\t"
      "v_mfma_f32_16x16x32_bf16 a[60:63], v[188:191], v[124:127], a[60:63]\n\t"
      "ds_read_b64_tr_b16 v[246:247], v9 offset:22016\n\t"
      "v_mfma_f32_16x16x32_bf16 a[60:63], v[212:215], v[128:131], a[60:63]\n\t"
      "s_add_u32 m0, s37, 9216\n\t"
      "s_mov_b32 s40, 14680064\n\t"
      "v_mfma_f32_16x16x32_bf16 a[92:95], v[188:191], v[128:131], a[92:95]\n\t"
      "buffer_load_dwordx4 v2, s[20:23], s40 offen offset:0 lds\n\t"
      "v_mfma_f32_16x16x32_bf16 a[92:95], v[212:215], v[132:135], a[92:95]\n\t"
      "v_mfma_f32_16x16x32_bf16 v[72:75], v[188:191], v[132:135], v[72:75]\n\t"
      "v_mfma_f32_16x16x32_bf16 v[72:75], v[212:215], v[136:139], v[72:75]\n\t"
      "s_waitcnt lgkmcnt(0)\n\t"
      "v_mfma_f32_16x16x32_bf16 a[0:3], v[228:231], v[112:115], a[0:3]\n\t"
      "buffer_load_dwordx4 v3, s[20:23], s40 offen offset:1024 lds\n\t"
      "v_mfma_f32_16x16x32_bf16 a[0:3], v[236:239], v[116:119], a[0:3]\n\t"
      "v_mfma_f32_16x16x32_bf16 a[32:35], v[228:231], v[116:119], a[32:35]\n\t"
      "v_mfma_f32_16x16x32_bf16 a[32:35], v[236:239], v[120:123], a[32:35]\n\t"
      "v_mfma_f32_16x16x32_bf16 a[64:67], v[228:231], v[120:123], a[64:67]\n\t"
      "buffer_load_dwordx4 v4, s[20:23], s40 offen offset:2048 lds\n\t"
      "v_mfma_f32_16x16x32_bf16 a[64:67], v[236:239], v[124:127], a[64:67]\n\t"
      "v_mfma_f32_16x16x32_bf16 v[44:47], v[228:231], v[124:127], v[44:47]\n\t"
      "v_mfma_f32_16x16x32_bf16 v[44:47], v[236:239], v[128:131], v[44:47]\n\t"
      "v_mfma_f32_16x16x32_bf16 a[16:19], v[228:231], v[128:131], a[16:19]\n\t"
      "buffer_load_dwordx4 v5, s[20:23], s40 offen offset:3072 lds\n\t"
      "v_mfma_f32_16x16x32_bf16 a[16:19], v[236:239], v[132:135], a[16:19]\n\t"
      "v_mfma_f32_16x16x32_bf16 a[48:51], v[228:231], v[132:135], a[48:51]\n\t"
      "v_mfma_f32_16x16x32_bf16 a[48:51], v[236:239], v[136:139], a[48:51]\n\t"
      "s_mov_b32 m0, s101\n\t"
      "v_mfma_f32_16x16x32_bf16 a[80:83], v[228:231], v[136:139], a[80:83]\n\t"
      "buffer_load_dwordx4 v27, s[16:19], s52 offen offset:0 lds\n\t"
      "v_mfma_f32_16x16x32_bf16 a[80:83], v[236:239], v[140:143], a[80:83]\n\t"
      "v_mfma_f32_16x16x32_bf16 v[60:63], v[228:231], v[140:143], v[60:63]\n\t"
      "v_mfma_f32_16x16x32_bf16 v[60:63], v[236:239], v[144:147], v[60:63]\n\t"
      "s_mov_b32 m0, s92\n\t"
      "v_mfma_f32_16x16x32_bf16 a[4:7], v[216:219], v[112:115], a[4:7]\n\t"
      "buffer_load_dwordx4 v28, s[16:19], s52 offen offset:0 lds\n\t"
      "v_mfma_f32_16x16x32_bf16 a[4:7], v[224:227], v[116:119], a[4:7]\n\t"
      "v_mfma_f32_16x16x32_bf16 a[36:39], v[216:219], v[116:119], a[36:39]\n\t"
      "v_mfma_f32_16x16x32_bf16 a[36:39], v[224:227], v[120:123], a[36:39]\n\t"
      "v_mfma_f32_16x16x32_bf16 a[68:71], v[216:219], v[120:123], a[68:71]\n\t"
      "buffer_load_dwordx4 v29, s[16:19], s52 offen offset:1024 lds\n\t"
      "v_mfma_f32_16x16x32_bf16 a[68:71], v[224:227], v[124:127], a[68:71]\n\t"
      "v_mfma_f32_16x16x32_bf16 v[48:51], v[216:219], v[124:127], v[48:51]\n\t"
      "v_mfma_f32_16x16x32_bf16 v[48:51], v[224:227], v[128:131], v[48:51]\n\t"
      "v_mfma_f32_16x16x32_bf16 a[20:23], v[216:219], v[128:131], a[20:23]\n\t"
      "buffer_load_dwordx4 v30, s[16:19], s52 offen offset:2048 lds\n\t"
      "v_mfma_f32_16x16x32_bf16 a[20:23], v[224:227], v[132:135], a[20:23]\n\t"
      "v_mfma_f32_16x16x32_bf16 a[52:55], v[216:219], v[132:135], a[52:55]\n\t"
      "v_mfma_f32_16x16x32_bf16 a[52:55], v[224:227], v[136:139], a[52:55]\n\t"
      "s_add_u32 m0, s37, 13312\n\t"
      "s_mov_b32 s40, 262144\n\t"
      "v_mfma_f32_16x16x32_bf16 a[84:87], v[216:219], v[136:139], a[84:87]\n\t"
      "buffer_load_dwordx4 v2, s[20:23], s40 offen offset:0 lds\n\t"
      "v_mfma_f32_16x16x32_bf16 a[84:87], v[224:227], v[140:143], a[84:87]\n\t"
      "s_waitcnt vmcnt(25)\n\t"
      "ds_read_b64_tr_b16 v[168:169], v6 offset:25600\n\t"
      "v_mfma_f32_16x16x32_bf16 v[64:67], v[216:219], v[140:143], v[64:67]\n\t"
      "ds_read_b64_tr_b16 v[170:171], v6 offset:26112\n\t"
      "v_mfma_f32_16x16x32_bf16 v[64:67], v[224:227], v[144:147], v[64:67]\n\t"
      "ds_read_b64_tr_b16 v[248:249], v7 offset:25600\n\t"
      "v_mfma_f32_16x16x32_bf16 a[8:11], v[232:235], v[112:115], a[8:11]\n\t"
      "ds_read_b64_tr_b16 v[250:251], v7 offset:26112\n\t"
      "buffer_load_dwordx4 v3, s[20:23], s40 offen offset:1024 lds\n\t"
      "v_mfma_f32_16x16x32_bf16 a[8:11], v[240:243], v[116:119], a[8:11]\n\t"
      "ds_read_b64_tr_b16 v[172:173], v8 offset:25600\n\t"
      "v_mfma_f32_16x16x32_bf16 a[40:43], v[232:235], v[116:119], a[40:43]\n\t"
      "ds_read_b64_tr_b16 v[174:175], v8 offset:26112\n\t"
      "v_mfma_f32_16x16x32_bf16 a[40:43], v[240:243], v[120:123], a[40:43]\n\t"
      "ds_read_b64_tr_b16 v[252:253], v9 offset:25600\n\t"
      "v_mfma_f32_16x16x32_bf16 a[72:75], v[232:235], v[120:123], a[72:75]\n\t"
      "ds_read_b64_tr_b16 v[254:255], v9 offset:26112\n\t"
      "buffer_load_dwordx4 v4, s[20:23], s40 offen offset:2048 lds\n\t"
      "v_mfma_f32_16x16x32_bf16 a[72:75], v[240:243], v[124:127], a[72:75]\n\t"
      "v_mfma_f32_16x16x32_bf16 v[52:55], v[232:235], v[124:127], v[52:55]\n\t"
      "v_mfma_f32_16x16x32_bf16 v[52:55], v[240:243], v[128:131], v[52:55]\n\t"
      "v_mfma_f32_16x16x32_bf16 a[24:27], v[232:235], v[128:131], a[24:27]\n\t"
      "buffer_load_dwordx4 v5, s[20:23], s40 offen offset:3072 lds\n\t"
      "v_mfma_f32_16x16x32_bf16 a[24:27], v[240:243], v[132:135], a[24:27]\n\t"
      "v_mfma_f32_16x16x32_bf16 a[56:59], v[232:235], v[132:135], a[56:59]\n\t"
      "v_mfma_f32_16x16x32_bf16 a[56:59], v[240:243], v[136:139], a[56:59]\n\t"
      "s_add_u32 m0, s37, 17408\n\t"
      "s_mov_b32 s40, 1310720\n\t"
      "v_mfma_f32_16x16x32_bf16 a[88:91], v[232:235], v[136:139], a[88:91]\n\t"
      "buffer_load_dwordx4 v2, s[20:23], s40 offen offset:0 lds\n\t"
      "v_mfma_f32_16x16x32_bf16 a[88:91], v[240:243], v[140:143], a[88:91]\n\t"
      "v_mfma_f32_16x16x32_bf16 v[68:71], v[232:235], v[140:143], v[68:71]\n\t"
      "v_mfma_f32_16x16x32_bf16 v[68:71], v[240:243], v[144:147], v[68:71]\n\t"
      "v_mfma_f32_16x16x32_bf16 a[12:15], v[220:223], v[112:115], a[12:15]\n\t"
      "buffer_load_dwordx4 v3, s[20:23], s40 offen offset:1024 lds\n\t"
      "v_mfma_f32_16x16x32_bf16 a[12:15], v[244:247], v[116:119], a[12:15]\n\t"
      "v_mfma_f32_16x16x32_bf16 a[44:47], v[220:223], v[116:119], a[44:47]\n\t"
      "v_mfma_f32_16x16x32_bf16 a[44:47], v[244:247], v[120:123], a[44:47]\n\t"
      "s_waitcnt vmcnt(18)\n\t"
      "ds_read_b64_tr_b16 v[176:177], v6 offset:29696\n\t"
      "v_mfma_f32_16x16x32_bf16 a[76:79], v[220:223], v[120:123], a[76:79]\n\t"
      "ds_read_b64_tr_b16 v[178:179], v6 offset:30208\n\t"
      "buffer_load_dwordx4 v4, s[20:23], s40 offen offset:2048 lds\n\t"
      "v_mfma_f32_16x16x32_bf16 a[76:79], v[244:247], v[124:127], a[76:79]\n\t"
      "ds_read_b64_tr_b16 v[164:165], v7 offset:29696\n\t"
      "v_mfma_f32_16x16x32_bf16 v[56:59], v[220:223], v[124:127], v[56:59]\n\t"
      "ds_read_b64_tr_b16 v[166:167], v7 offset:30208\n\t"
      "v_mfma_f32_16x16x32_bf16 v[56:59], v[244:247], v[128:131], v[56:59]\n\t"
      "ds_read_b64_tr_b16 v[180:181], v8 offset:29696\n\t"
      "v_mfma_f32_16x16x32_bf16 a[28:31], v[220:223], v[128:131], a[28:31]\n\t"
      "ds_read_b64_tr_b16 v[182:183], v8 offset:30208\n\t"
      "buffer_load_dwordx4 v5, s[20:23], s40 offen offset:3072 lds\n\t"
      "v_mfma_f32_16x16x32_bf16 a[28:31], v[244:247], v[132:135], a[28:31]\n\t"
      "ds_read_b64_tr_b16 v[196:197], v9 offset:29696\n\t"
      "v_mfma_f32_16x16x32_bf16 a[60:63], v[220:223], v[132:135], a[60:63]\n\t"
      "ds_read_b64_tr_b16 v[198:199], v9 offset:30208\n\t"
      "v_mfma_f32_16x16x32_bf16 a[60:63], v[244:247], v[136:139], a[60:63]\n\t"
      "s_add_u32 m0, s37, 21504\n\t"
      "s_mov_b32 s40, 2359296\n\t"
      "v_mfma_f32_16x16x32_bf16 a[92:95], v[220:223], v[136:139], a[92:95]\n\t"
      "buffer_load_dwordx4 v2, s[20:23], s40 offen offset:0 lds\n\t"
      "v_mfma_f32_16x16x32_bf16 a[92:95], v[244:247], v[140:143], a[92:95]\n\t"
      "v_mfma_f32_16x16x32_bf16 v[72:75], v[220:223], v[140:143], v[72:75]\n\t"
      "v_mfma_f32_16x16x32_bf16 v[72:75], v[244:247], v[144:147], v[72:75]\n\t"
      "s_waitcnt lgkmcnt(0)\n\t"
      "v_mfma_f32_16x16x32_bf16 a[0:3], v[168:171], v[120:123], a[0:3]\n\t"
      "buffer_load_dwordx4 v3, s[20:23], s40 offen offset:1024 lds\n\t"
      "v_mfma_f32_16x16x32_bf16 a[0:3], v[176:179], v[124:127], a[0:3]\n\t"
      "v_mfma_f32_16x16x32_bf16 a[32:35], v[168:171], v[124:127], a[32:35]\n\t"
      "s_waitcnt vmcnt(10)\n\t"
      "ds_read_b128 a[96:99], v10 offset:0\n\t"
      "v_mfma_f32_16x16x32_bf16 a[32:35], v[176:179], v[128:131], a[32:35]\n\t"
      "v_mfma_f32_16x16x32_bf16 a[64:67], v[168:171], v[128:131], a[64:67]\n\t"
      "buffer_load_dwordx4 v4, s[20:23], s40 offen offset:2048 lds\n\t"
      "v_mfma_f32_16x16x32_bf16 a[64:67], v[176:179], v[132:135], a[64:67]\n\t"
      "v_mfma_f32_16x16x32_bf16 v[44:47], v[168:171], v[132:135], v[44:47]\n\t"
      "ds_read_b128 a[100:103], v11 offset:0\n\t"
      "v_mfma_f32_16x16x32_bf16 v[44:47], v[176:179], v[136:139], v[44:47]\n\t"
      "v_mfma_f32_16x16x32_bf16 a[16:19], v[168:171], v[136:139], a[16:19]\n\t"
      "buffer_load_dwordx4 v5, s[20:23], s40 offen offset:3072 lds\n\t"
      "v_mfma_f32_16x16x32_bf16 a[16:19], v[176:179], v[140:143], a[16:19]\n\t"
      "v_mfma_f32_16x16x32_bf16 a[48:51], v[168:171], v[140:143], a[48:51]\n\t"
      "ds_read_b128 a[104:107], v12 offset:0\n\t"
      "v_mfma_f32_16x16x32_bf16 a[48:51], v[176:179], v[144:147], a[48:51]\n\t"
      "s_add_u32 m0, s37, 25600\n\t"
      "s_mov_b32 s40, 3407872\n\t"
      "v_mfma_f32_16x16x32_bf16 a[80:83], v[168:171], v[144:147], a[80:83]\n\t"
      "buffer_load_dwordx4 v2, s[20:23], s40 offen offset:0 lds\n\t"
      "v_mfma_f32_16x16x32_bf16 a[80:83], v[176:179], v[148:151], a[80:83]\n\t"
      "v_mfma_f32_16x16x32_bf16 v[60:63], v[168:171], v[148:151], v[60:63]\n\t"
      "ds_read_b128 a[108:111], v13 offset:0\n\t"
      "v_mfma_f32_16x16x32_bf16 v[60:63], v[176:179], v[152:155], v[60:63]\n\t"
      "v_mfma_f32_16x16x32_bf16 a[4:7], v[248:251], v[120:123], a[4:7]\n\t"
      "buffer_load_dwordx4 v3, s[20:23], s40 offen offset:1024 lds\n\t"
      "v_mfma_f32_16x16x32_bf16 a[4:7], v[164:167], v[124:127], a[4:7]\n\t"
      "v_mfma_f32_16x16x32_bf16 a[36:39], v[248:251], v[124:127], a[36:39]\n\t"
      "ds_read_b128 a[112:115], v10 offset:256\n\t"
      "v_mfma_f32_16x16x32_bf16 a[36:39], v[164:167], v[128:131], a[36:39]\n\t"
      "v_mfma_f32_16x16x32_bf16 a[68:71], v[248:251], v[128:131], a[68:71]\n\t"
      "buffer_load_dwordx4 v4, s[20:23], s40 offen offset:2048 lds\n\t"
      "v_mfma_f32_16x16x32_bf16 a[68:71], v[164:167], v[132:135], a[68:71]\n\t"
      "v_mfma_f32_16x16x32_bf16 v[48:51], v[248:251], v[132:135], v[48:51]\n\t"
      "ds_read_b128 a[116:119], v11 offset:256\n\t"
      "v_mfma_f32_16x16x32_bf16 v[48:51], v[164:167], v[136:139], v[48:51]\n\t"
      "v_mfma_f32_16x16x32_bf16 a[20:23], v[248:251], v[136:139], a[20:23]\n\t"
      "buffer_load_dwordx4 v5, s[20:23], s40 offen offset:3072 lds\n\t"
      "v_mfma_f32_16x16x32_bf16 a[20:23], v[164:167], v[140:143], a[20:23]\n\t"
      "v_mfma_f32_16x16x32_bf16 a[52:55], v[248:251], v[140:143], a[52:55]\n\t"
      "ds_read_b128 a[120:123], v12 offset:256\n\t"
      "v_mfma_f32_16x16x32_bf16 a[52:55], v[164:167], v[144:147], a[52:55]\n\t"
      "s_add_u32 m0, s37, 29696\n\t"
      "s_mov_b32 s40, 4456448\n\t"
      "v_mfma_f32_16x16x32_bf16 a[84:87], v[248:251], v[144:147], a[84:87]\n\t"
      "ds_read_b64_tr_b16 v[204:205], v6 offset:33792\n\t"
      "buffer_load_dwordx4 v2, s[20:23], s40 offen offset:0 lds\n\t"
      "v_mfma_f32_16x16x32_bf16 a[84:87], v[164:167], v[148:151], a[84:87]\n\t"
      "ds_read_b64_tr_b16 v[206:207], v6 offset:34304\n\t"
      "v_mfma_f32_16x16x32_bf16 v[64:67], v[248:251], v[148:151], v[64:67]\n\t"
      "ds_read_b64_tr_b16 v[184:185], v7 offset:33792\n\t"
      "v_mfma_f32_16x16x32_bf16 v[64:67], v[164:167], v[152:155], v[64:67]\n\t"
      "ds_read_b64_tr_b16 v[186:187], v7 offset:34304\n\t"
      "v_mfma_f32_16x16x32_bf16 a[8:11], v[172:175], v[120:123], a[8:11]\n\t"
      "ds_read_b64_tr_b16 v[192:193], v8 offset:33792\n\t"
      "buffer_load_dwordx4 v3, s[20:23], s40 offen offset:1024 lds\n\t"
      "v_mfma_f32_16x16x32_bf16 a[8:11], v[180:183], v[124:127], a[8:11]\n\t"
      "ds_read_b64_tr_b16 v[194:195], v8 offset:34304\n\t"
      "v_mfma_f32_16x16x32_bf16 a[40:43], v[172:175], v[124:127], a[40:43]\n\t"
      "ds_read_b64_tr_b16 v[200:201], v9 offset:33792\n\t"
      "v_mfma_f32_16x16x32_bf16 a[40:43], v[180:183], v[128:131], a[40:43]\n\t"
      "ds_read_b64_tr_b16 v[202:203], v9 offset:34304\n\t"
      "v_mfma_f32_16x16x32_bf16 a[72:75], v[172:175], v[128:131], a[72:75]\n\t"
      "ds_read_b128 a[124:127], v13 offset:256\n\t"
      "buffer_load_dwordx4 v4, s[20:23], s40 offen offset:2048 lds\n\t"
      "v_mfma_f32_16x16x32_bf16 a[72:75], v[180:183], v[132:135], a[72:75]\n\t"
      "v_mfma_f32_16x16x32_bf16 v[52:55], v[172:175], v[132:135], v[52:55]\n\t"
      "v_mfma_f32_16x16x32_bf16 v[52:55], v[180:183], v[136:139], v[52:55]\n\t"
      "v_mfma_f32_16x16x32_bf16 a[24:27], v[172:175], v[136:139], a[24:27]\n\t"
      "ds_read_b128 a[128:131], v14 offset:0\n\t"
      "buffer_load_dwordx4 v5, s[20:23], s40 offen offset:3072 lds\n\t"
      "v_mfma_f32_16x16x32_bf16 a[24:27], v[180:183], v[140:143], a[24:27]\n\t"
      "v_mfma_f32_16x16x32_bf16 a[56:59], v[172:175], v[140:143], a[56:59]\n\t"
      "v_mfma_f32_16x16x32_bf16 a[56:59], v[180:183], v[144:147], a[56:59]\n\t"
      "s_add_u32 m0, s37, 33792\n\t"
      "s_mov_b32 s40, 5505024\n\t"
      "v_mfma_f32_16x16x32_bf16 a[88:91], v[172:175], v[144:147], a[88:91]\n\t"
      "ds_read_b128 a[132:135], v15 offset:0\n\t"
      "v_mfma_f32_16x16x32_bf16 a[88:91], v[180:183], v[148:151], a[88:91]\n\t"
      "s_waitcnt lgkmcnt(1)\n\t"
      "buffer_load_dwordx4 v2, s[20:23], s40 offen offset:0 lds\n\t"
      "v_mfma_f32_16x16x32_bf16 v[68:71], v[172:175], v[148:151], v[68:71]\n\t"
      "v_mfma_f32_16x16x32_bf16 v[68:71], v[180:183], v[152:155], v[68:71]\n\t"
      "v_mfma_f32_16x16x32_bf16 a[12:15], v[252:255], v[120:123], a[12:15]\n\t"
      "ds_read_b128 a[136:139], v16 offset:0\n\t"
      "v_mfma_f32_16x16x32_bf16 a[12:15], v[196:199], v[124:127], a[12:15]\n\t"
      "buffer_load_dwordx4 v3, s[20:23], s40 offen offset:1024 lds\n\t"
      "v_mfma_f32_16x16x32_bf16 a[44:47], v[252:255], v[124:127], a[44:47]\n\t"
      "ds_read_b64_tr_b16 v[208:209], v6 offset:9216\n\t"
      "v_mfma_f32_16x16x32_bf16 a[44:47], v[196:199], v[128:131], a[44:47]\n\t"
      "ds_read_b64_tr_b16 v[210:211], v6 offset:9728\n\t"
      "v_mfma_f32_16x16x32_bf16 a[76:79], v[252:255], v[128:131], a[76:79]\n\t"
      "ds_read_b64_tr_b16 v[188:189], v7 offset:9216\n\t"
      "v_mfma_f32_16x16x32_bf16 a[76:79], v[196:199], v[132:135], a[76:79]\n\t"
      "ds_read_b64_tr_b16 v[190:191], v7 offset:9728\n\t"
      "v_mfma_f32_16x16x32_bf16 v[56:59], v[252:255], v[132:135], v[56:59]\n\t"
      "ds_read_b64_tr_b16 v[212:213], v8 offset:9216\n\t"
      "v_mfma_f32_16x16x32_bf16 v[56:59], v[196:199], v[136:139], v[56:59]\n\t"
      "ds_read_b64_tr_b16 v[214:215], v8 offset:9728\n\t"
      "v_mfma_f32_16x16x32_bf16 a[28:31], v[252:255], v[136:139], a[28:31]\n\t"
      "ds_read_b64_tr_b16 v[228:229], v9 offset:9216\n\t"
      "v_mfma_f32_16x16x32_bf16 a[28:31], v[196:199], v[140:143], a[28:31]\n\t"
      "ds_read_b64_tr_b16 v[230:231], v9 offset:9728\n\t"
      "v_mfma_f32_16x16x32_bf16 a[60:63], v[252:255], v[140:143], a[60:63]\n\t"
      "ds_read_b128 a[140:143], v17 offset:0\n\t"
      "v_mfma_f32_16x16x32_bf16 a[60:63], v[196:199], v[144:147], a[60:63]\n\t"
      "buffer_load_dwordx4 v4, s[20:23], s40 offen offset:2048 lds\n\t"
      "v_mfma_f32_16x16x32_bf16 a[92:95], v[252:255], v[144:147], a[92:95]\n\t"
      "v_mfma_f32_16x16x32_bf16 a[92:95], v[196:199], v[148:151], a[92:95]\n\t"
      "v_mfma_f32_16x16x32_bf16 v[72:75], v[252:255], v[148:151], v[72:75]\n\t"
      "ds_read_b128 a[144:147], v14 offset:256\n\t"
      "v_mfma_f32_16x16x32_bf16 v[72:75], v[196:199], v[152:155], v[72:75]\n\t"
      "buffer_load_dwordx4 v5, s[20:23], s40 offen offset:3072 lds\n\t"
      "v_mfma_f32_16x16x32_bf16 a[0:3], v[204:207], v[128:131], a[0:3]\n\t"
      "s_waitcnt lgkmcnt(1)\n\t"
      "v_mfma_f32_16x16x32_bf16 a[0:3], v[208:211], v[132:135], a[0:3]\n\t"
      "v_mfma_f32_16x16x32_bf16 a[32:35], v[204:207], v[132:135], a[32:35]\n\t"
      "ds_read_b128 a[148:151], v15 offset:256\n\t"
      "v_mfma_f32_16x16x32_bf16 a[32:35], v[208:211], v[136:139], a[32:35]\n\t"
      "s_add_u32 m0, s37, 9216\n\t"
      "s_mov_b32 s40, 6553600\n\t"
      "v_mfma_f32_16x16x32_bf16 a[64:67], v[204:207], v[136:139], a[64:67]\n\t"
      "s_barrier\n\t"
      "v_mfma_f32_16x16x32_bf16 a[64:67], v[208:211], v[140:143], a[64:67]\n\t"
      "s_mov_b32 s57, 7602176\n\t"
      "buffer_load_dword v35, v33, s[20:23], s57 offen\n\t"
      "v_mfma_f32_16x16x32_bf16 v[44:47], v[204:207], v[140:143], v[44:47]\n\t"
      "ds_read_b128 a[152:155], v16 offset:256\n\t"
      "v_mfma_f32_16x16x32_bf16 v[44:47], v[208:211], v[144:147], v[44:47]\n\t"
      "s_mov_b32 s57, 524288\n\t"
      "s_mov_b32 exec_lo, 0xfff\n\t"
      "s_mov_b32 exec_hi, 0x0\n\t"
      "buffer_load_dword v35, v33, s[20:23], s57 offen\n\t"
      "s_mov_b64 exec, -1\n\t"
      "v_mfma_f32_16x16x32_bf16 a[16:19], v[204:207], v[144:147], a[16:19]\n\t"
      "s_waitcnt vmcnt(22)\n\t"
      "ds_read_b64_tr_b16 v[236:237], v6 offset:13312\n\t"
      "v_mfma_f32_16x16x32_bf16 a[16:19], v[208:211], v[148:151], a[16:19]\n\t"
      "ds_read_b64_tr_b16 v[238:239], v6 offset:13824\n\t"
      "v_mfma_f32_16x16x32_bf16 a[48:51], v[204:207], v[148:151], a[48:51]\n\t"
      "ds_read_b64_tr_b16 v[216:217], v7 offset:13312\n\t"
      "v_mfma_f32_16x16x32_bf16 a[48:51], v[208:211], v[152:155], a[48:51]\n\t"
      "ds_read_b64_tr_b16 v[218:219], v7 offset:13824\n\t"
      "v_mfma_f32_16x16x32_bf16 a[80:83], v[204:207], v[152:155], a[80:83]\n\t"
      "ds_read_b64_tr_b16 v[224:225], v8 offset:13312\n\t"
      "v_mfma_f32_16x16x32_bf16 a[80:83], v[208:211], v[156:159], a[80:83]\n\t"
      "ds_read_b64_tr_b16 v[226:227], v8 offset:13824\n\t"
      "v_mfma_f32_16x16x32_bf16 v[60:63], v[204:207], v[156:159], v[60:63]\n\t"
      "ds_read_b64_tr_b16 v[232:233], v9 offset:13312\n\t"
      "v_mfma_f32_16x16x32_bf16 v[60:63], v[208:211], v[160:163], v[60:63]\n\t"
      "ds_read_b64_tr_b16 v[234:235], v9 offset:13824\n\t"
      "v_mfma_f32_16x16x32_bf16 a[4:7], v[184:187], v[128:131], a[4:7]\n\t"
      "ds_read_b128 a[156:159], v17 offset:256\n\t"
      "v_mfma_f32_16x16x32_bf16 a[4:7], v[188:191], v[132:135], a[4:7]\n\t"
      "s_mov_b64 exec, 1\n\t"
      "buffer_load_dword v37, off, s[28:31], s47 offset:4 sc1\n\t"
      "buffer_load_dword v40, off, s[28:31], s46 sc1\n\t"
      "s_mov_b64 exec, -1\n\t"
      "v_mfma_f32_16x16x32_bf16 a[36:39], v[184:187], v[132:135], a[36:39]\n\t"
      "v_mfma_f32_16x16x32_bf16 a[36:39], v[188:191], v[136:139], a[36:39]\n\t"
      "v_mfma_f32_16x16x32_bf16 a[68:71], v[184:187], v[136:139], a[68:71]\n\t"
      "ds_read_b128 a[160:163], v18 offset:0\n\t"
      "v_mfma_f32_16x16x32_bf16 a[68:71], v[188:191], v[140:143], a[68:71]\n\t"
      "v_mfma_f32_16x16x32_bf16 v[48:51], v[184:187], v[140:143], v[48:51]\n\t"
      "v_mfma_f32_16x16x32_bf16 v[48:51], v[188:191], v[144:147], v[48:51]\n\t"
      "v_mfma_f32_16x16x32_bf16 a[20:23], v[184:187], v[144:147], a[20:23]\n\t"
      "ds_read_b128 a[164:167], v19 offset:0\n\t"
      "v_mfma_f32_16x16x32_bf16 a[20:23], v[188:191], v[148:151], a[20:23]\n\t"
      "v_mfma_f32_16x16x32_bf16 a[52:55], v[184:187], v[148:151], a[52:55]\n\t"
      "v_mfma_f32_16x16x32_bf16 a[52:55], v[188:191], v[152:155], a[52:55]\n\t"
      "v_mfma_f32_16x16x32_bf16 a[84:87], v[184:187], v[152:155], a[84:87]\n\t"
      "ds_read_b128 a[168:171], v20 offset:0\n\t"
      "v_mfma_f32_16x16x32_bf16 a[84:87], v[188:191], v[156:159], a[84:87]\n\t"
      "v_mfma_f32_16x16x32_bf16 v[64:67], v[184:187], v[156:159], v[64:67]\n\t"
      "s_waitcnt vmcnt(20)\n\t"
      "ds_read_b64_tr_b16 v[240:241], v6 offset:17408\n\t"
      "v_mfma_f32_16x16x32_bf16 v[64:67], v[188:191], v[160:163], v[64:67]\n\t"
      "ds_read_b64_tr_b16 v[242:243], v6 offset:17920\n\t"
      "v_mfma_f32_16x16x32_bf16 a[8:11], v[192:195], v[128:131], a[8:11]\n\t"
      "ds_read_b64_tr_b16 v[220:221], v7 offset:17408\n\t"
      "v_mfma_f32_16x16x32_bf16 a[8:11], v[212:215], v[132:135], a[8:11]\n\t"
      "ds_read_b64_tr_b16 v[222:223], v7 offset:17920\n\t"
      "v_mfma_f32_16x16x32_bf16 a[40:43], v[192:195], v[132:135], a[40:43]\n\t"
      "ds_read_b64_tr_b16 v[244:245], v8 offset:17408\n\t"
      "v_mfma_f32_16x16x32_bf16 a[40:43], v[212:215], v[136:139], a[40:43]\n\t"
      "ds_read_b64_tr_b16 v[246:247], v8 offset:17920\n\t"
      "v_mfma_f32_16x16x32_bf16 a[72:75], v[192:195], v[136:139], a[72:75]\n\t"
      "ds_read_b64_tr_b16 v[168:169], v9 offset:17408\n\t"
      "v_mfma_f32_16x16x32_bf16 a[72:75], v[212:215], v[140:143], a[72:75]\n\t"
      "ds_read_b64_tr_b16 v[170:171], v9 offset:17920\n\t"
      "v_mfma_f32_16x16x32_bf16 v[52:55], v[192:195], v[140:143], v[52:55]\n\t"
      "ds_read_b128 a[172:175], v21 offset:0\n\t"
      "v_mfma_f32_16x16x32_bf16 v[52:55], v[212:215], v[144:147], v[52:55]\n\t"
      "v_mfma_f32_16x16x32_bf16 a[24:27], v[192:195], v[144:147], a[24:27]\n\t"
      "v_mfma_f32_16x16x32_bf16 a[24:27], v[212:215], v[148:151], a[24:27]\n\t"
      "v_mfma_f32_16x16x32_bf16 a[56:59], v[192:195], v[148:151], a[56:59]\n\t"
      "ds_read_b128 a[176:179], v18 offset:256\n\t"
      "v_mfma_f32_16x16x32_bf16 a[56:59], v[212:215], v[152:155], a[56:59]\n\t"
      "v_mfma_f32_16x16x32_bf16 a[88:91], v[192:195], v[152:155], a[88:91]\n\t"
      "v_mfma_f32_16x16x32_bf16 a[88:91], v[212:215], v[156:159], a[88:91]\n\t"
      "v_mfma_f32_16x16x32_bf16 v[68:71], v[192:195], v[156:159], v[68:71]\n\t"
      "ds_read_b128 a[180:183], v19 offset:256\n\t"
      "v_mfma_f32_16x16x32_bf16 v[68:71], v[212:215], v[160:163], v[68:71]\n\t"
      "v_mfma_f32_16x16x32_bf16 a[12:15], v[200:203], v[128:131], a[12:15]\n\t"
      "v_mfma_f32_16x16x32_bf16 a[12:15], v[228:231], v[132:135], a[12:15]\n\t"
      "v_mfma_f32_16x16x32_bf16 a[44:47], v[200:203], v[132:135], a[44:47]\n\t"
      "v_mfma_f32_16x16x32_bf16 a[44:47], v[228:231], v[136:139], a[44:47]\n\t"
      "v_mfma_f32_16x16x32_bf16 a[76:79], v[200:203], v[136:139], a[76:79]\n\t"
      "s_waitcnt vmcnt(16)\n\t"
      "ds_read_b64_tr_b16 v[176:177], v6 offset:21504\n\t"
      "v_mfma_f32_16x16x32_bf16 a[76:79], v[228:231], v[140:143], a[76:79]\n\t"
      "ds_read_b64_tr_b16 v[178:179], v6 offset:22016\n\t"
      "v_mfma_f32_16x16x32_bf16 v[56:59], v[200:203], v[140:143], v[56:59]\n\t"
      "ds_read_b64_tr_b16 v[248:249], v7 offset:21504\n\t"
      "v_mfma_f32_16x16x32_bf16 v[56:59], v[228:231], v[144:147], v[56:59]\n\t"
      "ds_read_b64_tr_b16 v[250:251], v7 offset:22016\n\t"
      "v_mfma_f32_16x16x32_bf16 a[28:31], v[200:203], v[144:147], a[28:31]\n\t"
      "ds_read_b64_tr_b16 v[164:165], v8 offset:21504\n\t"
      "v_mfma_f32_16x16x32_bf16 a[28:31], v[228:231], v[148:151], a[28:31]\n\t"
      "ds_read_b64_tr_b16 v[166:167], v8 offset:22016\n\t"
      "v_mfma_f32_16x16x32_bf16 a[60:63], v[200:203], v[148:151], a[60:63]\n\t"
      "ds_read_b64_tr_b16 v[172:173], v9 offset:21504\n\t"
      "v_mfma_f32_16x16x32_bf16 a[60:63], v[228:231], v[152:155], a[60:63]\n\t"
      "ds_read_b64_tr_b16 v[174:175], v9 offset:22016\n\t"
      "v_mfma_f32_16x16x32_bf16 a[92:95], v[200:203], v[152:155], a[92:95]\n\t"
      "v_mfma_f32_16x16x32_bf16 a[92:95], v[228:231], v[156:159], a[92:95]\n\t"
      "v_mfma_f32_16x16x32_bf16 v[72:75], v[200:203], v[156:159], v[72:75]\n\t"
      "v_mfma_f32_16x16x32_bf16 v[72:75], v[228:231], v[160:163], v[72:75]\n\t"
      "s_waitcnt lgkmcnt(1)\n\t"
      "v_mfma_f32_16x16x32_bf16 a[0:3], v[236:239], a[96:99], a[0:3]\n\t"
      "v_mfma_f32_16x16x32_bf16 a[0:3], v[240:243], a[100:103], a[0:3]\n\t"
      "v_mfma_f32_16x16x32_bf16 a[0:3], v[176:179], a[104:107], a[0:3]\n\t"
      "v_mfma_f32_16x16x32_bf16 a[32:35], v[236:239], a[100:103], a[32:35]\n\t"
      "v_mfma_f32_16x16x32_bf16 a[32:35], v[240:243], a[104:107], a[32:35]\n\t"
      "v_mfma_f32_16x16x32_bf16 a[32:35], v[176:179], a[108:111], a[32:35]\n\t"
      "v_mfma_f32_16x16x32_bf16 a[64:67], v[236:239], a[104:107], a[64:67]\n\t"
      "v_mfma_f32_16x16x32_bf16 a[64:67], v[240:243], a[108:111], a[64:67]\n\t"
      "v_mfma_f32_16x16x32_bf16 a[64:67], v[176:179], a[112:115], a[64:67]\n\t"
      "v_mfma_f32_16x16x32_bf16 v[44:47], v[236:239], a[108:111], v[44:47]\n\t"
      "v_mfma_f32_16x16x32_bf16 v[44:47], v[240:243], a[112:115], v[44:47]\n\t"
      "v_mfma_f32_16x16x32_bf16 v[44:47], v[176:179], a[116:119], v[44:47]\n\t"
      "v_mfma_f32_16x16x32_bf16 a[16:19], v[236:239], a[112:115], a[16:19]\n\t"
      "v_mfma_f32_16x16x32_bf16 a[16:19], v[240:243], a[116:119], a[16:19]\n\t"
      "v_mfma_f32_16x16x32_bf16 a[16:19], v[176:179], a[120:123], a[16:19]\n\t"
      "v_mfma_f32_16x16x32_bf16 a[48:51], v[236:239], a[116:119], a[48:51]\n\t"
      "v_mfma_f32_16x16x32_bf16 a[48:51], v[240:243], a[120:123], a[48:51]\n\t"
      "v_mfma_f32_16x16x32_bf16 a[48:51], v[176:179], a[124:127], a[48:51]\n\t"
      "v_mfma_f32_16x16x32_bf16 a[80:83], v[236:239], a[120:123], a[80:83]\n\t"
      "v_mfma_f32_16x16x32_bf16 a[80:83], v[240:243], a[124:127], a[80:83]\n\t"
      "v_mfma_f32_16x16x32_bf16 a[80:83], v[176:179], a[128:131], a[80:83]\n\t"
      "v_mfma_f32_16x16x32_bf16 v[60:63], v[236:239], a[124:127], v[60:63]\n\t"
      "v_mfma_f32_16x16x32_bf16 v[60:63], v[240:243], a[128:131], v[60:63]\n\t"
      "v_mfma_f32_16x16x32_bf16 v[60:63], v[176:179], a[132:135], v[60:63]\n\t"
      "v_mfma_f32_16x16x32_bf16 a[4:7], v[216:219], a[96:99], a[4:7]\n\t"
      "v_mfma_f32_16x16x32_bf16 a[4:7], v[220:223], a[100:103], a[4:7]\n\t"
      "v_mfma_f32_16x16x32_bf16 a[4:7], v[248:251], a[104:107], a[4:7]\n\t"
      "v_mfma_f32_16x16x32_bf16 a[36:39], v[216:219], a[100:103], a[36:39]\n\t"
      "v_mfma_f32_16x16x32_bf16 a[36:39], v[220:223], a[104:107], a[36:39]\n\t"
      "v_mfma_f32_16x16x32_bf16 a[36:39], v[248:251], a[108:111], a[36:39]\n\t"
      "v_mfma_f32_16x16x32_bf16 a[68:71], v[216:219], a[104:107], a[68:71]\n\t"
      "v_mfma_f32_16x16x32_bf16 a[68:71], v[220:223], a[108:111], a[68:71]\n\t"
      "v_mfma_f32_16x16x32_bf16 a[68:71], v[248:251], a[112:115], a[68:71]\n\t"
      "v_mfma_f32_16x16x32_bf16 v[48:51], v[216:219], a[108:111], v[48:51]\n\t"
      "v_mfma_f32_16x16x32_bf16 v[48:51], v[220:223], a[112:115], v[48:51]\n\t"
      "v_mfma_f32_16x16x32_bf16 v[48:51], v[248:251], a[116:119], v[48:51]\n\t"
      "v_mfma_f32_16x16x32_bf16 a[20:23], v[216:219], a[112:115], a[20:23]\n\t"
      "v_mfma_f32_16x16x32_bf16 a[20:23], v[220:223], a[116:119], a[20:23]\n\t"
      "v_mfma_f32_16x16x32_bf16 a[20:23], v[248:251], a[120:123], a[20:23]\n\t"
      "v_mfma_f32_16x16x32_bf16 a[52:55], v[216:219], a[116:119], a[52:55]\n\t"
      "v_mfma_f32_16x16x32_bf16 a[52:55], v[220:223], a[120:123], a[52:55]\n\t"
      "buffer_load_dwordx4 v2, s[20:23], s40 offen offset:0 lds\n\t"
      "v_mfma_f32_16x16x32_bf16 a[52:55], v[248:251], a[124:127], a[52:55]\n\t"
      "v_mfma_f32_16x16x32_bf16 a[84:87], v[216:219], a[120:123], a[84:87]\n\t"
      "v_mfma_f32_16x16x32_bf16 a[84:87], v[220:223], a[124:127], a[84:87]\n\t"
      "v_mfma_f32_16x16x32_bf16 a[84:87], v[248:251], a[128:131], a[84:87]\n\t"
      "buffer_load_dwordx4 v3, s[20:23], s40 offen offset:1024 lds\n\t"
      "v_mfma_f32_16x16x32_bf16 v[64:67], v[216:219], a[124:127], v[64:67]\n\t"
      "v_mfma_f32_16x16x32_bf16 v[64:67], v[220:223], a[128:131], v[64:67]\n\t"
      "v_mfma_f32_16x16x32_bf16 v[64:67], v[248:251], a[132:135], v[64:67]\n\t"
      "v_mfma_f32_16x16x32_bf16 a[8:11], v[224:227], a[96:99], a[8:11]\n\t"
      "buffer_load_dwordx4 v4, s[20:23], s40 offen offset:2048 lds\n\t"
      "v_mfma_f32_16x16x32_bf16 a[8:11], v[244:247], a[100:103], a[8:11]\n\t"
      "v_mfma_f32_16x16x32_bf16 a[8:11], v[164:167], a[104:107], a[8:11]\n\t"
      "v_mfma_f32_16x16x32_bf16 a[40:43], v[224:227], a[100:103], a[40:43]\n\t"
      "v_mfma_f32_16x16x32_bf16 a[40:43], v[244:247], a[104:107], a[40:43]\n\t"
      "buffer_load_dwordx4 v5, s[20:23], s40 offen offset:3072 lds\n\t"
      "v_mfma_f32_16x16x32_bf16 a[40:43], v[164:167], a[108:111], a[40:43]\n\t"
      "v_mfma_f32_16x16x32_bf16 a[72:75], v[224:227], a[104:107], a[72:75]\n\t"
      "v_mfma_f32_16x16x32_bf16 a[72:75], v[244:247], a[108:111], a[72:75]\n\t"
      "s_add_u32 m0, s37, 13312\n\t"
      "s_mov_b32 s40, 7602176\n\t"
      "v_mfma_f32_16x16x32_bf16 a[72:75], v[164:167], a[112:115], a[72:75]\n\t"
      "buffer_load_dwordx4 v2, s[20:23], s40 offen offset:0 lds\n\t"
      "v_mfma_f32_16x16x32_bf16 v[52:55], v[224:227], a[108:111], v[52:55]\n\t"
      "v_mfma_f32_16x16x32_bf16 v[52:55], v[244:247], a[112:115], v[52:55]\n\t"
      "v_mfma_f32_16x16x32_bf16 v[52:55], v[164:167], a[116:119], v[52:55]\n\t"
      "v_mfma_f32_16x16x32_bf16 a[24:27], v[224:227], a[112:115], a[24:27]\n\t"
      "buffer_load_dwordx4 v3, s[20:23], s40 offen offset:1024 lds\n\t"
      "v_mfma_f32_16x16x32_bf16 a[24:27], v[244:247], a[116:119], a[24:27]\n\t"
      "s_waitcnt vmcnt(18)\n\t"
      "ds_read_b64_tr_b16 v[180:181], v6 offset:25600\n\t"
      "v_mfma_f32_16x16x32_bf16 a[24:27], v[164:167], a[120:123], a[24:27]\n\t"
      "ds_read_b64_tr_b16 v[182:183], v6 offset:26112\n\t"
      "v_mfma_f32_16x16x32_bf16 a[56:59], v[224:227], a[116:119], a[56:59]\n\t"
      "ds_read_b64_tr_b16 v[252:253], v7 offset:25600\n\t"
      "v_mfma_f32_16x16x32_bf16 a[56:59], v[244:247], a[120:123], a[56:59]\n\t"
      "ds_read_b64_tr_b16 v[254:255], v7 offset:26112\n\t"
      "buffer_load_dwordx4 v4, s[20:23], s40 offen offset:2048 lds\n\t"
      "v_mfma_f32_16x16x32_bf16 a[56:59], v[164:167], a[124:127], a[56:59]\n\t"
      "ds_read_b64_tr_b16 v[196:197], v8 offset:25600\n\t"
      "v_mfma_f32_16x16x32_bf16 a[88:91], v[224:227], a[120:123], a[88:91]\n\t"
      "ds_read_b64_tr_b16 v[198:199], v8 offset:26112\n\t"
      "v_mfma_f32_16x16x32_bf16 a[88:91], v[244:247], a[124:127], a[88:91]\n\t"
      "ds_read_b64_tr_b16 v[204:205], v9 offset:25600\n\t"
      "v_mfma_f32_16x16x32_bf16 a[88:91], v[164:167], a[128:131], a[88:91]\n\t"
      "ds_read_b64_tr_b16 v[206:207], v9 offset:26112\n\t"
      "buffer_load_dwordx4 v5, s[20:23], s40 offen offset:3072 lds\n\t"
      "v_mfma_f32_16x16x32_bf16 v[68:71], v[224:227], a[124:127], v[68:71]\n\t"
      "v_mfma_f32_16x16x32_bf16 v[68:71], v[244:247], a[128:131], v[68:71]\n\t"
      "v_mfma_f32_16x16x32_bf16 v[68:71], v[164:167], a[132:135], v[68:71]\n\t"
      "s_add_u32 m0, s37, 17408\n\t"
      "s_mov_b32 s40, 8650752\n\t"
      "v_mfma_f32_16x16x32_bf16 a[12:15], v[232:235], a[96:99], a[12:15]\n\t"
      "buffer_load_dwordx4 v2, s[20:23], s40 offen offset:0 lds\n\t"
      "v_mfma_f32_16x16x32_bf16 a[12:15], v[168:171], a[100:103], a[12:15]\n\t"
      "s_waitcnt lgkmcnt(0)\n\t"
      "v_mfma_f32_16x16x32_bf16 a[12:15], v[172:175], a[104:107], a[12:15]\n\t"
      "v_mfma_f32_16x16x32_bf16 a[44:47], v[232:235], a[100:103], a[44:47]\n\t"
      "v_mfma_f32_16x16x32_bf16 a[44:47], v[168:171], a[104:107], a[44:47]\n\t"
      "buffer_load_dwordx4 v3, s[20:23], s40 offen offset:1024 lds\n\t"
      "v_mfma_f32_16x16x32_bf16 a[44:47], v[172:175], a[108:111], a[44:47]\n\t"
      "v_mfma_f32_16x16x32_bf16 a[76:79], v[232:235], a[104:107], a[76:79]\n\t"
      "v_mfma_f32_16x16x32_bf16 a[76:79], v[168:171], a[108:111], a[76:79]\n\t"
      "v_mfma_f32_16x16x32_bf16 a[76:79], v[172:175], a[112:115], a[76:79]\n\t"
      "buffer_load_dwordx4 v4, s[20:23], s40 offen offset:2048 lds\n\t"
      "v_mfma_f32_16x16x32_bf16 v[56:59], v[232:235], a[108:111], v[56:59]\n\t"
      "v_mfma_f32_16x16x32_bf16 v[56:59], v[168:171], a[112:115], v[56:59]\n\t"
      "v_mfma_f32_16x16x32_bf16 v[56:59], v[172:175], a[116:119], v[56:59]\n\t"
      "s_waitcnt vmcnt(19)\n\t"
      "ds_read_b64_tr_b16 v[208:209], v6 offset:29696\n\t"
      "v_mfma_f32_16x16x32_bf16 a[28:31], v[232:235], a[112:115], a[28:31]\n\t"
      "ds_read_b64_tr_b16 v[210:211], v6 offset:30208\n\t"
      "buffer_load_dwordx4 v5, s[20:23], s40 offen offset:3072 lds\n\t"
      "v_mfma_f32_16x16x32_bf16 a[28:31], v[168:171], a[116:119], a[28:31]\n\t"
      "ds_read_b64_tr_b16 v[184:185], v7 offset:29696\n\t"
      "v_mfma_f32_16x16x32_bf16 a[28:31], v[172:175], a[120:123], a[28:31]\n\t"
      "ds_read_b64_tr_b16 v[186:187], v7 offset:30208\n\t"
      "v_mfma_f32_16x16x32_bf16 a[60:63], v[232:235], a[116:119], a[60:63]\n\t"
      "ds_read_b64_tr_b16 v[188:189], v8 offset:29696\n\t"
      "s_add_u32 m0, s37, 21504\n\t"
      "s_mov_b32 s40, 9699328\n\t"
      "v_mfma_f32_16x16x32_bf16 a[60:63], v[168:171], a[120:123], a[60:63]\n\t"
      "ds_read_b64_tr_b16 v[190:191], v8 offset:30208\n\t"
      "buffer_load_dwordx4 v2, s[20:23], s40 offen offset:0 lds\n\t"
      "v_mfma_f32_16x16x32_bf16 a[60:63], v[172:175], a[124:127], a[60:63]\n\t"
      "ds_read_b64_tr_b16 v[192:193], v9 offset:29696\n\t"
      "v_mfma_f32_16x16x32_bf16 a[92:95], v[232:235], a[120:123], a[92:95]\n\t"
      "ds_read_b64_tr_b16 v[194:195], v9 offset:30208\n\t"
      "v_mfma_f32_16x16x32_bf16 a[92:95], v[168:171], a[124:127], a[92:95]\n\t"
      "v_mfma_f32_16x16x32_bf16 a[92:95], v[172:175], a[128:131], a[92:95]\n\t"
      "buffer_load_dwordx4 v3, s[20:23], s40 offen offset:1024 lds\n\t"
      "v_mfma_f32_16x16x32_bf16 v[72:75], v[232:235], a[124:127], v[72:75]\n\t"
      "v_mfma_f32_16x16x32_bf16 v[72:75], v[168:171], a[128:131], v[72:75]\n\t"
      "v_mfma_f32_16x16x32_bf16 v[72:75], v[172:175], a[132:135], v[72:75]\n\t"
      "v_mfma_f32_16x16x32_bf16 a[0:3], v[180:183], a[108:111], a[0:3]\n\t"
      "buffer_load_dwordx4 v4, s[20:23], s40 offen offset:2048 lds\n\t"
      "s_waitcnt lgkmcnt(0)\n\t"
      "v_mfma_f32_16x16x32_bf16 a[0:3], v[208:211], a[112:115], a[0:3]\n\t"
      "v_mfma_f32_16x16x32_bf16 a[32:35], v[180:183], a[112:115], a[32:35]\n\t"
      "v_mfma_f32_16x16x32_bf16 a[32:35], v[208:211], a[116:119], a[32:35]\n\t"
      "v_mfma_f32_16x16x32_bf16 a[64:67], v[180:183], a[116:119], a[64:67]\n\t"
      "buffer_load_dwordx4 v5, s[20:23], s40 offen offset:3072 lds\n\t"
      "v_mfma_f32_16x16x32_bf16 a[64:67], v[208:211], a[120:123], a[64:67]\n\t"
      "v_mfma_f32_16x16x32_bf16 v[44:47], v[180:183], a[120:123], v[44:47]\n\t"
      "v_mfma_f32_16x16x32_bf16 v[44:47], v[208:211], a[124:127], v[44:47]\n\t"
      "s_add_u32 m0, s37, 25600\n\t"
      "s_mov_b32 s40, 10747904\n\t"
      "v_mfma_f32_16x16x32_bf16 a[16:19], v[180:183], a[124:127], a[16:19]\n\t"
      "buffer_load_dwordx4 v2, s[20:23], s40 offen offset:0 lds\n\t"
      "v_mfma_f32_16x16x32_bf16 a[16:19], v[208:211], a[128:131], a[16:19]\n\t"
      "v_mfma_f32_16x16x32_bf16 a[48:51], v[180:183], a[128:131], a[48:51]\n\t"
      "v_mfma_f32_16x16x32_bf16 a[48:51], v[208:211], a[132:135], a[48:51]\n\t"
      "v_mfma_f32_16x16x32_bf16 a[80:83], v[180:183], a[132:135], a[80:83]\n\t"
      "buffer_load_dwordx4 v3, s[20:23], s40 offen offset:1024 lds\n\t"
      "v_mfma_f32_16x16x32_bf16 a[80:83], v[208:211], a[136:139], a[80:83]\n\t"
      "v_mfma_f32_16x16x32_bf16 v[60:63], v[180:183], a[136:139], v[60:63]\n\t"
      "v_mfma_f32_16x16x32_bf16 v[60:63], v[208:211], a[140:143], v[60:63]\n\t"
      "v_mfma_f32_16x16x32_bf16 a[4:7], v[252:255], a[108:111], a[4:7]\n\t"
      "buffer_load_dwordx4 v4, s[20:23], s40 offen offset:2048 lds\n\t"
      "v_mfma_f32_16x16x32_bf16 a[4:7], v[184:187], a[112:115], a[4:7]\n\t"
      "v_mfma_f32_16x16x32_bf16 a[36:39], v[252:255], a[112:115], a[36:39]\n\t"
      "v_mfma_f32_16x16x32_bf16 a[36:39], v[184:187], a[116:119], a[36:39]\n\t"
      "v_mfma_f32_16x16x32_bf16 a[68:71], v[252:255], a[116:119], a[68:71]\n\t"
      "buffer_load_dwordx4 v5, s[20:23], s40 offen offset:3072 lds\n\t"
      "v_mfma_f32_16x16x32_bf16 a[68:71], v[184:187], a[120:123], a[68:71]\n\t"
      "v_mfma_f32_16x16x32_bf16 v[48:51], v[252:255], a[120:123], v[48:51]\n\t"
      "v_mfma_f32_16x16x32_bf16 v[48:51], v[184:187], a[124:127], v[48:51]\n\t"
      "s_add_u32 m0, s37, 29696\n\t"
      "s_mov_b32 s40, 11796480\n\t"
      "v_mfma_f32_16x16x32_bf16 a[20:23], v[252:255], a[124:127], a[20:23]\n\t"
      "buffer_load_dwordx4 v2, s[20:23], s40 offen offset:0 lds\n\t"
      "v_mfma_f32_16x16x32_bf16 a[20:23], v[184:187], a[128:131], a[20:23]\n\t"
      "v_mfma_f32_16x16x32_bf16 a[52:55], v[252:255], a[128:131], a[52:55]\n\t"
      "s_barrier\n\t"
      "v_mfma_f32_16x16x32_bf16 a[52:55], v[184:187], a[132:135], a[52:55]\n\t"
      "s_mov_b32 s57, 3670016\n\t"
      "s_mov_b32 exec_lo, 0xfffffff\n\t"
      "s_mov_b32 exec_hi, 0x0\n\t"
      "buffer_load_dword v35, v33, s[20:23], s57 offen\n\t"
      "s_mov_b64 exec, -1\n\t"
      "v_mfma_f32_16x16x32_bf16 a[84:87], v[252:255], a[132:135], a[84:87]\n\t"
      "s_mov_b32 m0, s59\n\t"
      "v_mfma_f32_16x16x32_bf16 a[84:87], v[184:187], a[136:139], a[84:87]\n\t"
      "s_waitcnt vmcnt(26)\n\t"
      "ds_read_b64_tr_b16 v[212:213], v6 offset:33792\n\t"
      "v_mfma_f32_16x16x32_bf16 v[64:67], v[252:255], a[136:139], v[64:67]\n\t"
      "ds_read_b64_tr_b16 v[214:215], v6 offset:34304\n\t"
      "v_mfma_f32_16x16x32_bf16 v[64:67], v[184:187], a[140:143], v[64:67]\n\t"
      "ds_read_b64_tr_b16 v[200:201], v7 offset:33792\n\t"
      "v_mfma_f32_16x16x32_bf16 a[8:11], v[196:199], a[108:111], a[8:11]\n\t"
      "ds_read_b64_tr_b16 v[202:203], v7 offset:34304\n\t"
      "v_mfma_f32_16x16x32_bf16 a[8:11], v[188:191], a[112:115], a[8:11]\n\t"
      "ds_read_b64_tr_b16 v[228:229], v8 offset:33792\n\t"
      "v_mfma_f32_16x16x32_bf16 a[40:43], v[196:199], a[112:115], a[40:43]\n\t"
      "ds_read_b64_tr_b16 v[230:231], v8 offset:34304\n\t"
      "v_mfma_f32_16x16x32_bf16 a[40:43], v[188:191], a[116:119], a[40:43]\n\t"
      "ds_read_b64_tr_b16 v[236:237], v9 offset:33792\n\t"
      "v_mfma_f32_16x16x32_bf16 a[72:75], v[196:199], a[116:119], a[72:75]\n\t"
      "ds_read_b64_tr_b16 v[238:239], v9 offset:34304\n\t"
      "v_mfma_f32_16x16x32_bf16 a[72:75], v[188:191], a[120:123], a[72:75]\n\t"
      "buffer_load_dwordx4 v22, s[16:19], s53 offen offset:0 lds\n\t"
      "v_mfma_f32_16x16x32_bf16 v[52:55], v[196:199], a[120:123], v[52:55]\n\t"
      "v_mfma_f32_16x16x32_bf16 v[52:55], v[188:191], a[124:127], v[52:55]\n\t"
      "v_mfma_f32_16x16x32_bf16 a[24:27], v[196:199], a[124:127], a[24:27]\n\t"
      "s_mov_b32 m0, s100\n\t"
      "v_mfma_f32_16x16x32_bf16 a[24:27], v[188:191], a[128:131], a[24:27]\n\t"
      "buffer_load_dwordx4 v23, s[16:19], s53 offen offset:0 lds\n\t"
      "v_mfma_f32_16x16x32_bf16 a[56:59], v[196:199], a[128:131], a[56:59]\n\t"
      "v_mfma_f32_16x16x32_bf16 a[56:59], v[188:191], a[132:135], a[56:59]\n\t"
      "v_mfma_f32_16x16x32_bf16 a[88:91], v[196:199], a[132:135], a[88:91]\n\t"
      "v_mfma_f32_16x16x32_bf16 a[88:91], v[188:191], a[136:139], a[88:91]\n\t"
      "buffer_load_dwordx4 v24, s[16:19], s53 offen offset:1024 lds\n\t"
      "v_mfma_f32_16x16x32_bf16 v[68:71], v[196:199], a[136:139], v[68:71]\n\t"
      "v_mfma_f32_16x16x32_bf16 v[68:71], v[188:191], a[140:143], v[68:71]\n\t"
      "v_mfma_f32_16x16x32_bf16 a[12:15], v[204:207], a[108:111], a[12:15]\n\t"
      "v_mfma_f32_16x16x32_bf16 a[12:15], v[192:195], a[112:115], a[12:15]\n\t"
      "buffer_load_dwordx4 v25, s[16:19], s53 offen offset:2048 lds\n\t"
      "v_mfma_f32_16x16x32_bf16 a[44:47], v[204:207], a[112:115], a[44:47]\n\t"
      "v_mfma_f32_16x16x32_bf16 a[44:47], v[192:195], a[116:119], a[44:47]\n\t"
      "s_waitcnt vmcnt(22)\n\t"
      "ds_read_b64_tr_b16 v[240:241], v6 offset:9216\n\t"
      "v_mfma_f32_16x16x32_bf16 a[76:79], v[204:207], a[116:119], a[76:79]\n\t"
      "ds_read_b64_tr_b16 v[242:243], v6 offset:9728\n\t"
      "s_add_u32 m0, s37, 8192\n\t"
      "v_mfma_f32_16x16x32_bf16 a[76:79], v[192:195], a[120:123], a[76:79]\n\t"
      "ds_read_b64_tr_b16 v[176:177], v7 offset:9216\n\t"
      "v_mfma_f32_16x16x32_bf16 v[56:59], v[204:207], a[120:123], v[56:59]\n\t"
      "ds_read_b64_tr_b16 v[178:179], v7 offset:9728\n\t"
      "v_mfma_f32_16x16x32_bf16 v[56:59], v[192:195], a[124:127], v[56:59]\n\t"
      "ds_read_b64_tr_b16 v[216:217], v8 offset:9216\n\t"
      "v_mfma_f32_16x16x32_bf16 a[28:31], v[204:207], a[124:127], a[28:31]\n\t"
      "ds_read_b64_tr_b16 v[218:219], v8 offset:9728\n\t"
      "v_mfma_f32_16x16x32_bf16 a[28:31], v[192:195], a[128:131], a[28:31]\n\t"
      "ds_read_b64_tr_b16 v[220:221], v9 offset:9216\n\t"
      "v_mfma_f32_16x16x32_bf16 a[60:63], v[204:207], a[128:131], a[60:63]\n\t"
      "ds_read_b64_tr_b16 v[222:223], v9 offset:9728\n\t"
      "v_mfma_f32_16x16x32_bf16 a[60:63], v[192:195], a[132:135], a[60:63]\n\t"
      "buffer_load_dwordx4 v26, s[16:19], s53 offen offset:0 lds\n\t"
      "v_mfma_f32_16x16x32_bf16 a[92:95], v[204:207], a[132:135], a[92:95]\n\t"
      "v_mfma_f32_16x16x32_bf16 a[92:95], v[192:195], a[136:139], a[92:95]\n\t"
      "v_mfma_f32_16x16x32_bf16 v[72:75], v[204:207], a[136:139], v[72:75]\n\t"
      "v_mfma_f32_16x16x32_bf16 v[72:75], v[192:195], a[140:143], v[72:75]\n\t"
      "v_mov_b32 v248, -1\n\t"
      "v_mov_b32 v249, -1\n\t"
      "v_mov_b32 v250, -1\n\t"
      "v_mov_b32 v251, -1\n\t"
      "buffer_store_dwordx4 v[248:251], v31, s[28:31], s44 offen offset:0 sc1\n\t"
      "s_waitcnt lgkmcnt(0)\n\t"
      "v_mfma_f32_16x16x32_bf16 a[0:3], v[212:215], a[116:119], a[0:3]\n\t"
      "buffer_store_dwordx4 v[248:251], v31, s[28:31], s44 offen offset:1024 sc1\n\t"
      "v_mfma_f32_16x16x32_bf16 a[0:3], v[240:243], a[120:123], a[0:3]\n\t"
      "v_add_u32 v37, 1, v37\n\t"
      "v_cmp_eq_u32 vcc, v37, v40\n\t"
      "s_mov_b64 exec, 1\n\t"
      "s_nop 0\n\t"
      "v_addc_co_u32 v37, vcc, 0, v37, vcc\n\t"
      "buffer_store_dword v37, off, s[28:31], s46 sc1\n\t"
      "s_mov_b64 exec, -1\n\t"
      "v_mfma_f32_16x16x32_bf16 a[32:35], v[212:215], a[120:123], a[32:35]\n\t"
      "v_mfma_f32_16x16x32_bf16 a[32:35], v[240:243], a[124:127], a[32:35]\n\t"
      "v_mfma_f32_16x16x32_bf16 a[64:67], v[212:215], a[124:127], a[64:67]\n\t"
      "v_mfma_f32_16x16x32_bf16 a[64:67], v[240:243], a[128:131], a[64:67]\n\t"
      "v_mfma_f32_16x16x32_bf16 v[44:47], v[212:215], a[128:131], v[44:47]\n\t"
      "v_mfma_f32_16x16x32_bf16 v[44:47], v[240:243], a[132:135], v[44:47]\n\t"
      "v_mfma_f32_16x16x32_bf16 a[16:19], v[212:215], a[132:135], a[16:19]\n\t"
      "v_mfma_f32_16x16x32_bf16 a[16:19], v[240:243], a[136:139], a[16:19]\n\t"
      "v_mfma_f32_16x16x32_bf16 a[48:51], v[212:215], a[136:139], a[48:51]\n\t"
      "v_mfma_f32_16x16x32_bf16 a[48:51], v[240:243], a[140:143], a[48:51]\n\t"
      "v_mfma_f32_16x16x32_bf16 a[80:83], v[212:215], a[140:143], a[80:83]\n\t"
      "v_mfma_f32_16x16x32_bf16 a[80:83], v[240:243], a[144:147], a[80:83]\n\t"
      "v_mfma_f32_16x16x32_bf16 v[60:63], v[212:215], a[144:147], v[60:63]\n\t"
      "v_mfma_f32_16x16x32_bf16 v[60:63], v[240:243], a[148:151], v[60:63]\n\t"
      "v_mfma_f32_16x16x32_bf16 a[4:7], v[200:203], a[116:119], a[4:7]\n\t"
      "v_mfma_f32_16x16x32_bf16 a[4:7], v[176:179], a[120:123], a[4:7]\n\t"
      "v_mfma_f32_16x16x32_bf16 a[36:39], v[200:203], a[120:123], a[36:39]\n\t"
      "v_mfma_f32_16x16x32_bf16 a[36:39], v[176:179], a[124:127], a[36:39]\n\t"
      "v_mfma_f32_16x16x32_bf16 a[68:71], v[200:203], a[124:127], a[68:71]\n\t"
      "v_mfma_f32_16x16x32_bf16 a[68:71], v[176:179], a[128:131], a[68:71]\n\t"
      "v_mfma_f32_16x16x32_bf16 v[48:51], v[200:203], a[128:131], v[48:51]\n\t"
      "v_mfma_f32_16x16x32_bf16 v[48:51], v[176:179], a[132:135], v[48:51]\n\t"
      "v_mfma_f32_16x16x32_bf16 a[20:23], v[200:203], a[132:135], a[20:23]\n\t"
      "v_mfma_f32_16x16x32_bf16 a[20:23], v[176:179], a[136:139], a[20:23]\n\t"
      "v_mfma_f32_16x16x32_bf16 a[52:55], v[200:203], a[136:139], a[52:55]\n\t"
      "v_mfma_f32_16x16x32_bf16 a[52:55], v[176:179], a[140:143], a[52:55]\n\t"
      "v_mfma_f32_16x16x32_bf16 a[84:87], v[200:203], a[140:143], a[84:87]\n\t"
      "v_mfma_f32_16x16x32_bf16 a[84:87], v[176:179], a[144:147], a[84:87]\n\t"
      "s_waitcnt vmcnt(22)\n\t"
      "ds_read_b64_tr_b16 v[224:225], v6 offset:13312\n\t"
      "v_mfma_f32_16x16x32_bf16 v[64:67], v[200:203], a[144:147], v[64:67]\n\t"
      "ds_read_b64_tr_b16 v[226:227], v6 offset:13824\n\t"
      "v_mfma_f32_16x16x32_bf16 v[64:67], v[176:179], a[148:151], v[64:67]\n\t"
      "ds_read_b64_tr_b16 v[244:245], v7 offset:13312\n\t"
      "v_mfma_f32_16x16x32_bf16 a[8:11], v[228:231], a[116:119], a[8:11]\n\t"
      "ds_read_b64_tr_b16 v[246:247], v7 offset:13824\n\t"
      "v_mfma_f32_16x16x32_bf16 a[8:11], v[216:219], a[120:123], a[8:11]\n\t"
      "ds_read_b64_tr_b16 v[164:165], v8 offset:13312\n\t"
      "v_mfma_f32_16x16x32_bf16 a[40:43], v[228:231], a[120:123], a[40:43]\n\t"
      "ds_read_b64_tr_b16 v[166:167], v8 offset:13824\n\t"
      "v_mfma_f32_16x16x32_bf16 a[40:43], v[216:219], a[124:127], a[40:43]\n\t"
      "ds_read_b64_tr_b16 v[232:233], v9 offset:13312\n\t"
      "v_mfma_f32_16x16x32_bf16 a[72:75], v[228:231], a[124:127], a[72:75]\n\t"
      "ds_read_b64_tr_b16 v[234:235], v9 offset:13824\n\t"
      "v_mfma_f32_16x16x32_bf16 a[72:75], v[216:219], a[128:131], a[72:75]\n\t"
      "v_mfma_f32_16x16x32_bf16 v[52:55], v[228:231], a[128:131], v[52:55]\n\t"
      "v_mfma_f32_16x16x32_bf16 v[52:55], v[216:219], a[132:135], v[52:55]\n\t"
      "v_mfma_f32_16x16x32_bf16 a[24:27], v[228:231], a[132:135], a[24:27]\n\t"
      "v_mfma_f32_16x16x32_bf16 a[24:27], v[216:219], a[136:139], a[24:27]\n\t"
      "v_mfma_f32_16x16x32_bf16 a[56:59], v[228:231], a[136:139], a[56:59]\n\t"
      "v_mfma_f32_16x16x32_bf16 a[56:59], v[216:219], a[140:143], a[56:59]\n\t"
      "v_mfma_f32_16x16x32_bf16 a[88:91], v[228:231], a[140:143], a[88:91]\n\t"
      "v_mfma_f32_16x16x32_bf16 a[88:91], v[216:219], a[144:147], a[88:91]\n\t"
      "v_mfma_f32_16x16x32_bf16 v[68:71], v[228:231], a[144:147], v[68:71]\n\t"
      "v_mfma_f32_16x16x32_bf16 v[68:71], v[216:219], a[148:151], v[68:71]\n\t"
      "v_mfma_f32_16x16x32_bf16 a[12:15], v[236:239], a[116:119], a[12:15]\n\t"
      "v_mfma_f32_16x16x32_bf16 a[12:15], v[220:223], a[120:123], a[12:15]\n\t"
      "v_mfma_f32_16x16x32_bf16 a[44:47], v[236:239], a[120:123], a[44:47]\n\t"
      "v_mfma_f32_16x16x32_bf16 a[44:47], v[220:223], a[124:127], a[44:47]\n\t"
      "s_waitcnt vmcnt(18)\n\t"
      "ds_read_b64_tr_b16 v[168:169], v6 offset:17408\n\t"
      "v_mfma_f32_16x16x32_bf16 a[76:79], v[236:239], a[124:127], a[76:79]\n\t"
      "ds_read_b64_tr_b16 v[170:171], v6 offset:17920\n\t"
      "v_mfma_f32_16x16x32_bf16 a[76:79], v[220:223], a[128:131], a[76:79]\n\t"
      "ds_read_b64_tr_b16 v[172:173], v7 offset:17408\n\t"
      "v_mfma_f32_16x16x32_bf16 v[56:59], v[236:239], a[128:131], v[56:59]\n\t"
      "ds_read_b64_tr_b16 v[174:175], v7 offset:17920\n\t"
      "v_mfma_f32_16x16x32_bf16 v[56:59], v[220:223], a[132:135], v[56:59]\n\t"
      "ds_read_b64_tr_b16 v[180:181], v8 offset:17408\n\t"
      "v_mfma_f32_16x16x32_bf16 a[28:31], v[236:239], a[132:135], a[28:31]\n\t"
      "ds_read_b64_tr_b16 v[182:183], v8 offset:17920\n\t"
      "v_mfma_f32_16x16x32_bf16 a[28:31], v[220:223], a[136:139], a[28:31]\n\t"
      "ds_read_b64_tr_b16 v[208:209], v9 offset:17408\n\t"
      "v_mfma_f32_16x16x32_bf16 a[60:63], v[236:239], a[136:139], a[60:63]\n\t"
      "ds_read_b64_tr_b16 v[210:211], v9 offset:17920\n\t"
      "v_mfma_f32_16x16x32_bf16 a[60:63], v[220:223], a[140:143], a[60:63]\n\t"
      "v_mfma_f32_16x16x32_bf16 a[92:95], v[236:239], a[140:143], a[92:95]\n\t"
      "v_mfma_f32_16x16x32_bf16 a[92:95], v[220:223], a[144:147], a[92:95]\n\t"
      "v_mfma_f32_16x16x32_bf16 v[72:75], v[236:239], a[144:147], v[72:75]\n\t"
      "s_add_u32 m0, s37, 29696\n\t"
      "s_nop 0\n\t"
      "buffer_load_dwordx4 v3, s[20:23], s40 offen offset:1024 lds\n\t"
      "v_mfma_f32_16x16x32_bf16 v[72:75], v[220:223], a[148:151], v[72:75]\n\t"
      "s_waitcnt lgkmcnt(0)\n\t"
      "v_mfma_f32_16x16x32_bf16 a[0:3], v[224:227], a[124:127], a[0:3]\n\t"
      "v_mfma_f32_16x16x32_bf16 a[0:3], v[168:171], a[128:131], a[0:3]\n\t"
      "v_mfma_f32_16x16x32_bf16 a[32:35], v[224:227], a[128:131], a[32:35]\n\t"
      "buffer_load_dwordx4 v4, s[20:23], s40 offen offset:2048 lds\n\t"
      "v_mfma_f32_16x16x32_bf16 a[32:35], v[168:171], a[132:135], a[32:35]\n\t"
      "v_mfma_f32_16x16x32_bf16 a[64:67], v[224:227], a[132:135], a[64:67]\n\t"
      "v_mfma_f32_16x16x32_bf16 a[64:67], v[168:171], a[136:139], a[64:67]\n\t"
      "v_mfma_f32_16x16x32_bf16 v[44:47], v[224:227], a[136:139], v[44:47]\n\t"
      "buffer_load_dwordx4 v5, s[20:23], s40 offen offset:3072 lds\n\t"
      "v_mfma_f32_16x16x32_bf16 v[44:47], v[168:171], a[140:143], v[44:47]\n\t"
      "v_mfma_f32_16x16x32_bf16 a[16:19], v[224:227], a[140:143], a[16:19]\n\t"
      "v_mfma_f32_16x16x32_bf16 a[16:19], v[168:171], a[144:147], a[16:19]\n\t"
      "s_add_u32 m0, s37, 33792\n\t"
      "s_mov_b32 s40, 12845056\n\t"
      "v_mfma_f32_16x16x32_bf16 a[48:51], v[224:227], a[144:147], a[48:51]\n\t"
      "buffer_load_dwordx4 v2, s[20:23], s40 offen offset:0 lds\n\t"
      "v_mfma_f32_16x16x32_bf16 a[48:51], v[168:171], a[148:151], a[48:51]\n\t"
      "v_mfma_f32_16x16x32_bf16 a[80:83], v[224:227], a[148:151], a[80:83]\n\t"
      "v_mfma_f32_16x16x32_bf16 a[80:83], v[168:171], a[152:155], a[80:83]\n\t"
      "v_mfma_f32_16x16x32_bf16 v[60:63], v[224:227], a[152:155], v[60:63]\n\t"
      "buffer_load_dwordx4 v3, s[20:23], s40 offen offset:1024 lds\n\t"
      "v_mfma_f32_16x16x32_bf16 v[60:63], v[168:171], a[156:159], v[60:63]\n\t"
      "v_mfma_f32_16x16x32_bf16 a[4:7], v[244:247], a[124:127], a[4:7]\n\t"
      "v_mfma_f32_16x16x32_bf16 a[4:7], v[172:175], a[128:131], a[4:7]\n\t"
      "v_mfma_f32_16x16x32_bf16 a[36:39], v[244:247], a[128:131], a[36:39]\n\t"
      "buffer_load_dwordx4 v4, s[20:23], s40 offen offset:2048 lds\n\t"
      "v_mfma_f32_16x16x32_bf16 a[36:39], v[172:175], a[132:135], a[36:39]\n\t"
      "v_mfma_f32_16x16x32_bf16 a[68:71], v[244:247], a[132:135], a[68:71]\n\t"
      "v_mfma_f32_16x16x32_bf16 a[68:71], v[172:175], a[136:139], a[68:71]\n\t"
      "v_mfma_f32_16x16x32_bf16 v[48:51], v[244:247], a[136:139], v[48:51]\n\t"
      "buffer_load_dwordx4 v5, s[20:23], s40 offen offset:3072 lds\n\t"
      "v_mfma_f32_16x16x32_bf16 v[48:51], v[172:175], a[140:143], v[48:51]\n\t"
      "v_mfma_f32_16x16x32_bf16 a[20:23], v[244:247], a[140:143], a[20:23]\n\t"
      "v_mfma_f32_16x16x32_bf16 a[20:23], v[172:175], a[144:147], a[20:23]\n\t"
      "s_add_u32 m0, s37, 9216\n\t"
      "s_mov_b32 s40, 13893632\n\t"
      "v_mfma_f32_16x16x32_bf16 a[52:55], v[244:247], a[144:147], a[52:55]\n\t"
      "buffer_load_dwordx4 v2, s[20:23], s40 offen offset:0 lds\n\t"
      "v_mfma_f32_16x16x32_bf16 a[52:55], v[172:175], a[148:151], a[52:55]\n\t"
      "v_mfma_f32_16x16x32_bf16 a[84:87], v[244:247], a[148:151], a[84:87]\n\t"
      "v_mfma_f32_16x16x32_bf16 a[84:87], v[172:175], a[152:155], a[84:87]\n\t"
      "s_waitcnt vmcnt(22)\n\t"
      "ds_read_b64_tr_b16 v[252:253], v6 offset:21504\n\t"
      "v_mfma_f32_16x16x32_bf16 v[64:67], v[244:247], a[152:155], v[64:67]\n\t"
      "ds_read_b64_tr_b16 v[254:255], v6 offset:22016\n\t"
      "buffer_load_dwordx4 v3, s[20:23], s40 offen offset:1024 lds\n\t"
      "v_mfma_f32_16x16x32_bf16 v[64:67], v[172:175], a[156:159], v[64:67]\n\t"
      "ds_read_b64_tr_b16 v[184:185], v7 offset:21504\n\t"
      "v_mfma_f32_16x16x32_bf16 a[8:11], v[164:167], a[124:127], a[8:11]\n\t"
      "ds_read_b64_tr_b16 v[186:187], v7 offset:22016\n\t"
      "v_mfma_f32_16x16x32_bf16 a[8:11], v[180:183], a[128:131], a[8:11]\n\t"
      "ds_read_b64_tr_b16 v[196:197], v8 offset:21504\n\t"
      "v_mfma_f32_16x16x32_bf16 a[40:43], v[164:167], a[128:131], a[40:43]\n\t"
      "ds_read_b64_tr_b16 v[198:199], v8 offset:22016\n\t"
      "buffer_load_dwordx4 v4, s[20:23], s40 offen offset:2048 lds\n\t"
      "v_mfma_f32_16x16x32_bf16 a[40:43], v[180:183], a[132:135], a[40:43]\n\t"
      "ds_read_b64_tr_b16 v[188:189], v9 offset:21504\n\t"
      "v_mfma_f32_16x16x32_bf16 a[72:75], v[164:167], a[132:135], a[72:75]\n\t"
      "ds_read_b64_tr_b16 v[190:191], v9 offset:22016\n\t"
      "v_mfma_f32_16x16x32_bf16 a[72:75], v[180:183], a[136:139], a[72:75]\n\t"
      "v_mfma_f32_16x16x32_bf16 v[52:55], v[164:167], a[136:139], v[52:55]\n\t"
      "buffer_load_dwordx4 v5, s[20:23], s40 offen offset:3072 lds\n\t"
      "v_mfma_f32_16x16x32_bf16 v[52:55], v[180:183], a[140:143], v[52:55]\n\t"
      "v_mfma_f32_16x16x32_bf16 a[24:27], v[164:167], a[140:143], a[24:27]\n\t"
      "v_mfma_f32_16x16x32_bf16 a[24:27], v[180:183], a[144:147], a[24:27]\n\t"
      "s_add_u32 m0, s37, 13312\n\t"
      "s_mov_b32 s40, 14942208\n\t"
      "v_mfma_f32_16x16x32_bf16 a[56:59], v[164:167], a[144:147], a[56:59]\n\t"
      "buffer_load_dwordx4 v2, s[20:23], s40 offen offset:0 lds\n\t"
      "v_mfma_f32_16x16x32_bf16 a[56:59], v[180:183], a[148:151], a[56:59]\n\t"
      "v_mfma_f32_16x16x32_bf16 a[88:91], v[164:167], a[148:151], a[88:91]\n\t"
      "v_mfma_f32_16x16x32_bf16 a[88:91], v[180:183], a[152:155], a[88:91]\n\t"
      "v_mfma_f32_16x16x32_bf16 v[68:71], v[164:167], a[152:155], v[68:71]\n\t"
      "buffer_load_dwordx4 v3, s[20:23], s40 offen offset:1024 lds\n\t"
      "v_mfma_f32_16x16x32_bf16 v[68:71], v[180:183], a[156:159], v[68:71]\n\t"
      "v_mfma_f32_16x16x32_bf16 a[12:15], v[232:235], a[124:127], a[12:15]\n\t"
      "v_mfma_f32_16x16x32_bf16 a[12:15], v[208:211], a[128:131], a[12:15]\n\t"
      "v_mfma_f32_16x16x32_bf16 a[44:47], v[232:235], a[128:131], a[44:47]\n\t"
      "buffer_load_dwordx4 v4, s[20:23], s40 offen offset:2048 lds\n\t"
      "v_mfma_f32_16x16x32_bf16 a[44:47], v[208:211], a[132:135], a[44:47]\n\t"
      "s_waitcnt vmcnt(24)\n\t"
      "ds_read_b64_tr_b16 v[204:205], v6 offset:25600\n\t"
      "v_mfma_f32_16x16x32_bf16 a[76:79], v[232:235], a[132:135], a[76:79]\n\t"
      "ds_read_b64_tr_b16 v[206:207], v6 offset:26112\n\t"
      "v_mfma_f32_16x16x32_bf16 a[76:79], v[208:211], a[136:139], a[76:79]\n\t"
      "ds_read_b64_tr_b16 v[192:193], v7 offset:25600\n\t"
      "v_mfma_f32_16x16x32_bf16 v[56:59], v[232:235], a[136:139], v[56:59]\n\t"
      "ds_read_b64_tr_b16 v[194:195], v7 offset:26112\n\t"
      "buffer_load_dwordx4 v5, s[20:23], s40 offen offset:3072 lds\n\t"
      "v_mfma_f32_16x16x32_bf16 v[56:59], v[208:211], a[140:143], v[56:59]\n\t"
      "ds_read_b64_tr_b16 v[248:249], v8 offset:25600\n\t"
      "v_mfma_f32_16x16x32_bf16 a[28:31], v[232:235], a[140:143], a[28:31]\n\t"
      "ds_read_b64_tr_b16 v[250:251], v8 offset:26112\n\t"
      "v_mfma_f32_16x16x32_bf16 a[28:31], v[208:211], a[144:147], a[28:31]\n\t"
      "ds_read_b64_tr_b16 v[212:213], v9 offset:25600\n\t"
      "s_add_u32 m0, s37, 17408\n\t"
      "s_mov_b32 s40, 524288\n\t"
      "v_mfma_f32_16x16x32_bf16 a[60:63], v[232:235], a[144:147], a[60:63]\n\t"
      "ds_read_b64_tr_b16 v[214:215], v9 offset:26112\n\t"
      "buffer_load_dwordx4 v2, s[20:23], s40 offen offset:0 lds\n\t"
      "v_mfma_f32_16x16x32_bf16 a[60:63], v[208:211], a[148:151], a[60:63]\n\t"
      "v_mfma_f32_16x16x32_bf16 a[92:95], v[232:235], a[148:151], a[92:95]\n\t"
      "v_mfma_f32_16x16x32_bf16 a[92:95], v[208:211], a[152:155], a[92:95]\n\t"
      "v_mfma_f32_16x16x32_bf16 v[72:75], v[232:235], a[152:155], v[72:75]\n\t"
      "buffer_load_dwordx4 v3, s[20:23], s40 offen offset:1024 lds\n\t"
      "v_mfma_f32_16x16x32_bf16 v[72:75], v[208:211], a[156:159], v[72:75]\n\t"
      "s_waitcnt lgkmcnt(0)\n\t"
      "v_mfma_f32_16x16x32_bf16 a[0:3], v[252:255], a[132:135], a[0:3]\n\t"
      "v_mfma_f32_16x16x32_bf16 a[0:3], v[204:207], a[136:139], a[0:3]\n\t"
      "s_mov_b32 m0, s101\n\t"
      "v_mfma_f32_16x16x32_bf16 a[32:35], v[252:255], a[136:139], a[32:35]\n\t"
      "buffer_load_dwordx4 v27, s[16:19], s53 offen offset:0 lds\n\t"
      "v_mfma_f32_16x16x32_bf16 a[32:35], v[204:207], a[140:143], a[32:35]\n\t"
      "v_mfma_f32_16x16x32_bf16 a[64:67], v[252:255], a[140:143], a[64:67]\n\t"
      "v_mfma_f32_16x16x32_bf16 a[64:67], v[204:207], a[144:147], a[64:67]\n\t"
      "s_mov_b32 m0, s92\n\t"
      "v_mfma_f32_16x16x32_bf16 v[44:47], v[252:255], a[144:147], v[44:47]\n\t"
      "buffer_load_dwordx4 v28, s[16:19], s53 offen offset:0 lds\n\t"
      "v_mfma_f32_16x16x32_bf16 v[44:47], v[204:207], a[148:151], v[44:47]\n\t"
      "v_mfma_f32_16x16x32_bf16 a[16:19], v[252:255], a[148:151], a[16:19]\n\t"
      "v_mfma_f32_16x16x32_bf16 a[16:19], v[204:207], a[152:155], a[16:19]\n\t"
      "v_mfma_f32_16x16x32_bf16 a[48:51], v[252:255], a[152:155], a[48:51]\n\t"
      "buffer_load_dwordx4 v29, s[16:19], s53 offen offset:1024 lds\n\t"
      "v_mfma_f32_16x16x32_bf16 a[48:51], v[204:207], a[156:159], a[48:51]\n\t"
      "v_mfma_f32_16x16x32_bf16 a[80:83], v[252:255], a[156:159], a[80:83]\n\t"
      "v_mfma_f32_16x16x32_bf16 a[80:83], v[204:207], a[160:163], a[80:83]\n\t"
      "v_mfma_f32_16x16x32_bf16 v[60:63], v[252:255], a[160:163], v[60:63]\n\t"
      "buffer_load_dwordx4 v30, s[16:19], s53 offen offset:2048 lds\n\t"
      "v_mfma_f32_16x16x32_bf16 v[60:63], v[204:207], a[164:167], v[60:63]\n\t"
      "v_mfma_f32_16x16x32_bf16 a[4:7], v[184:187], a[132:135], a[4:7]\n\t"
      "v_mfma_f32_16x16x32_bf16 a[4:7], v[192:195], a[136:139], a[4:7]\n\t"
      "v_mfma_f32_16x16x32_bf16 a[36:39], v[184:187], a[136:139], a[36:39]\n\t"
      "s_add_u32 m0, s37, 17408\n\t"
      "s_nop 0\n\t"
      "buffer_load_dwordx4 v4, s[20:23], s40 offen offset:2048 lds\n\t"
      "v_mfma_f32_16x16x32_bf16 a[36:39], v[192:195], a[140:143], a[36:39]\n\t"
      "v_mfma_f32_16x16x32_bf16 a[68:71], v[184:187], a[140:143], a[68:71]\n\t"
      "v_mfma_f32_16x16x32_bf16 a[68:71], v[192:195], a[144:147], a[68:71]\n\t"
      "v_mfma_f32_16x16x32_bf16 v[48:51], v[184:187], a[144:147], v[48:51]\n\t"
      "buffer_load_dwordx4 v5, s[20:23], s40 offen offset:3072 lds\n\t"
      "v_mfma_f32_16x16x32_bf16 v[48:51], v[192:195], a[148:151], v[48:51]\n\t"
      "v_mfma_f32_16x16x32_bf16 a[20:23], v[184:187], a[148:151], a[20:23]\n\t"
      "v_mfma_f32_16x16x32_bf16 a[20:23], v[192:195], a[152:155], a[20:23]\n\t"
      "s_add_u32 m0, s37, 21504\n\t"
      "s_mov_b32 s40, 1572864\n\t"
      "v_mfma_f32_16x16x32_bf16 a[52:55], v[184:187], a[152:155], a[52:55]\n\t"
      "buffer_load_dwordx4 v2, s[20:23], s40 offen offset:0 lds\n\t"
      "v_mfma_f32_16x16x32_bf16 a[52:55], v[192:195], a[156:159], a[52:55]\n\t"
      "v_mfma_f32_16x16x32_bf16 a[84:87], v[184:187], a[156:159], a[84:87]\n\t"
      "v_mfma_f32_16x16x32_bf16 a[84:87], v[192:195], a[160:163], a[84:87]\n\t"
      "s_waitcnt vmcnt(21)\n\t"
      "ds_read_b64_tr_b16 v[240:241], v6 offset:29696\n\t"
      "v_mfma_f32_16x16x32_bf16 v[64:67], v[184:187], a[160:163], v[64:67]\n\t"
      "ds_read_b64_tr_b16 v[242:243], v6 offset:30208\n\t"
      "buffer_load_dwordx4 v3, s[20:23], s40 offen offset:1024 lds\n\t"
      "v_mfma_f32_16x16x32_bf16 v[64:67], v[192:195], a[164:167], v[64:67]\n\t"
      "ds_read_b64_tr_b16 v[200:201], v7 offset:29696\n\t"
      "v_mfma_f32_16x16x32_bf16 a[8:11], v[196:199], a[132:135], a[8:11]\n\t"
      "ds_read_b64_tr_b16 v[202:203], v7 offset:30208\n\t"
      "v_mfma_f32_16x16x32_bf16 a[8:11], v[248:251], a[136:139], a[8:11]\n\t"
      "ds_read_b64_tr_b16 v[176:177], v8 offset:29696\n\t"
      "v_mfma_f32_16x16x32_bf16 a[40:43], v[196:199], a[136:139], a[40:43]\n\t"
      "ds_read_b64_tr_b16 v[178:179], v8 offset:30208\n\t"
      "buffer_load_dwordx4 v4, s[20:23], s40 offen offset:2048 lds\n\t"
      "v_mfma_f32_16x16x32_bf16 a[40:43], v[248:251], a[140:143], a[40:43]\n\t"
      "ds_read_b64_tr_b16 v[228:229], v9 offset:29696\n\t"
      "v_mfma_f32_16x16x32_bf16 a[72:75], v[196:199], a[140:143], a[72:75]\n\t"
      "ds_read_b64_tr_b16 v[230:231], v9 offset:30208\n\t"
      "v_mfma_f32_16x16x32_bf16 a[72:75], v[248:251], a[144:147], a[72:75]\n\t"
      "v_mfma_f32_16x16x32_bf16 v[52:55], v[196:199], a[144:147], v[52:55]\n\t"
      "buffer_load_dwordx4 v5, s[20:23], s40 offen offset:3072 lds\n\t"
      "v_mfma_f32_16x16x32_bf16 v[52:55], v[248:251], a[148:151], v[52:55]\n\t"
      "v_mfma_f32_16x16x32_bf16 a[24:27], v[196:199], a[148:151], a[24:27]\n\t"
      "v_mfma_f32_16x16x32_bf16 a[24:27], v[248:251], a[152:155], a[24:27]\n\t"
      "s_add_u32 m0, s37, 25600\n\t"
      "s_mov_b32 s40, 2621440\n\t"
      "v_mfma_f32_16x16x32_bf16 a[56:59], v[196:199], a[152:155], a[56:59]\n\t"
      "buffer_load_dwordx4 v2, s[20:23], s40 offen offset:0 lds\n\t"
      "v_mfma_f32_16x16x32_bf16 a[56:59], v[248:251], a[156:159], a[56:59]\n\t"
      "v_mfma_f32_16x16x32_bf16 a[88:91], v[196:199], a[156:159], a[88:91]\n\t"
      "v_mfma_f32_16x16x32_bf16 a[88:91], v[248:251], a[160:163], a[88:91]\n\t"
      "v_mfma_f32_16x16x32_bf16 v[68:71], v[196:199], a[160:163], v[68:71]\n\t"
      "buffer_load_dwordx4 v3, s[20:23], s40 offen offset:1024 lds\n\t"
      "v_mfma_f32_16x16x32_bf16 v[68:71], v[248:251], a[164:167], v[68:71]\n\t"
      "v_mfma_f32_16x16x32_bf16 a[12:15], v[188:191], a[132:135], a[12:15]\n\t"
      "v_mfma_f32_16x16x32_bf16 a[12:15], v[212:215], a[136:139], a[12:15]\n\t"
      "v_mfma_f32_16x16x32_bf16 a[44:47], v[188:191], a[136:139], a[44:47]\n\t"
      "buffer_load_dwordx4 v4, s[20:23], s40 offen offset:2048 lds\n\t"
      "v_mfma_f32_16x16x32_bf16 a[44:47], v[212:215], a[140:143], a[44:47]\n\t"
      "s_waitcnt vmcnt(23)\n\t"
      "ds_read_b64_tr_b16 v[216:217], v6 offset:33792\n\t"
      "v_mfma_f32_16x16x32_bf16 a[76:79], v[188:191], a[140:143], a[76:79]\n\t"
      "ds_read_b64_tr_b16 v[218:219], v6 offset:34304\n\t"
      "v_mfma_f32_16x16x32_bf16 a[76:79], v[212:215], a[144:147], a[76:79]\n\t"
      "ds_read_b64_tr_b16 v[236:237], v7 offset:33792\n\t"
      "v_mfma_f32_16x16x32_bf16 v[56:59], v[188:191], a[144:147], v[56:59]\n\t"
      "ds_read_b64_tr_b16 v[238:239], v7 offset:34304\n\t"
      "buffer_load_dwordx4 v5, s[20:23], s40 offen offset:3072 lds\n\t"
      "v_mfma_f32_16x16x32_bf16 v[56:59], v[212:215], a[148:151], v[56:59]\n\t"
      "ds_read_b64_tr_b16 v[220:221], v8 offset:33792\n\t"
      "v_mfma_f32_16x16x32_bf16 a[28:31], v[188:191], a[148:151], a[28:31]\n\t"
      "ds_read_b64_tr_b16 v[222:223], v8 offset:34304\n\t"
      "v_mfma_f32_16x16x32_bf16 a[28:31], v[212:215], a[152:155], a[28:31]\n\t"
      "ds_read_b64_tr_b16 v[224:225], v9 offset:33792\n\t"
      "s_add_u32 m0, s37, 29696\n\t"
      "s_mov_b32 s40, 3670016\n\t"
      "v_mfma_f32_16x16x32_bf16 a[60:63], v[188:191], a[152:155], a[60:63]\n\t"
      "ds_read_b64_tr_b16 v[226:227], v9 offset:34304\n\t"
      "s_waitcnt lgkmcnt(5)\n\t"
      "buffer_load_dwordx4 v2, s[20:23], s40 offen offset:0 lds\n\t"
      "v_mfma_f32_16x16x32_bf16 a[60:63], v[212:215], a[156:159], a[60:63]\n\t"
      "v_mfma_f32_16x16x32_bf16 a[92:95], v[188:191], a[156:159], a[92:95]\n\t"
      "s_barrier\n\t"
      "v_mfma_f32_16x16x32_bf16 a[92:95], v[212:215], a[160:163], a[92:95]\n\t"
      "s_mov_b32 s57, 11010048\n\t"
      "buffer_load_dword v35, v33, s[20:23], s57 offen\n\t"
      "v_mfma_f32_16x16x32_bf16 v[72:75], v[188:191], a[160:163], v[72:75]\n\t"
      "s_mov_b32 s57, 786432\n\t"
      "s_mov_b32 exec_hi, 0x0\n\t"
      "buffer_load_dword v35, v33, s[20:23], s57 offen\n\t"
      "s_mov_b64 exec, -1\n\t"
      "v_mfma_f32_16x16x32_bf16 v[72:75], v[212:215], a[164:167], v[72:75]\n\t"
      "s_mov_b64 exec, 1\n\t"
      "buffer_load_dword v36, off, s[28:31], s47 sc1\n\t"
      "s_mov_b64 exec, -1\n\t"
      "v_mfma_f32_16x16x32_bf16 a[0:3], v[240:243], a[140:143], a[0:3]\n\t"
      "v_mfma_f32_16x16x32_bf16 a[0:3], v[216:219], a[144:147], a[0:3]\n\t"
      "v_mfma_f32_16x16x32_bf16 a[32:35], v[240:243], a[144:147], a[32:35]\n\t"
      "s_waitcnt vmcnt(14)\n\t"
      "ds_read_b128 v[76:79], v10 offset:0\n\t"
      "v_mfma_f32_16x16x32_bf16 a[32:35], v[216:219], a[148:151], a[32:35]\n\t"
      "v_mfma_f32_16x16x32_bf16 a[64:67], v[240:243], a[148:151], a[64:67]\n\t"
      "v_mfma_f32_16x16x32_bf16 a[64:67], v[216:219], a[152:155], a[64:67]\n\t"
      "v_mfma_f32_16x16x32_bf16 v[44:47], v[240:243], a[152:155], v[44:47]\n\t"
      "ds_read_b128 v[80:83], v11 offset:0\n\t"
      "v_mfma_f32_16x16x32_bf16 v[44:47], v[216:219], a[156:159], v[44:47]\n\t"
      "v_mfma_f32_16x16x32_bf16 a[16:19], v[240:243], a[156:159], a[16:19]\n\t"
      "v_mfma_f32_16x16x32_bf16 a[16:19], v[216:219], a[160:163], a[16:19]\n\t"
      "v_mfma_f32_16x16x32_bf16 a[48:51], v[240:243], a[160:163], a[48:51]\n\t"
      "ds_read_b128 v[84:87], v12 offset:0\n\t"
      "v_mfma_f32_16x16x32_bf16 a[48:51], v[216:219], a[164:167], a[48:51]\n\t"
      "v_mfma_f32_16x16x32_bf16 a[80:83], v[240:243], a[164:167], a[80:83]\n\t"
      "v_mfma_f32_16x16x32_bf16 a[80:83], v[216:219], a[168:171], a[80:83]\n\t"
      "v_mfma_f32_16x16x32_bf16 v[60:63], v[240:243], a[168:171], v[60:63]\n\t"
      "ds_read_b128 v[88:91], v13 offset:0\n\t"
      "v_mfma_f32_16x16x32_bf16 v[60:63], v[216:219], a[172:175], v[60:63]\n\t"
      "v_mfma_f32_16x16x32_bf16 a[4:7], v[200:203], a[140:143], a[4:7]\n\t"
      "s_waitcnt lgkmcnt(1)\n\t"
      "v_mfma_f32_16x16x32_bf16 a[4:7], v[236:239], a[144:147], a[4:7]\n\t"
      "v_mfma_f32_16x16x32_bf16 a[36:39], v[200:203], a[144:147], a[36:39]\n\t"
      "ds_read_b128 v[92:95], v10 offset:256\n\t"
      "v_mfma_f32_16x16x32_bf16 a[36:39], v[236:239], a[148:151], a[36:39]\n\t"
      "v_mfma_f32_16x16x32_bf16 a[68:71], v[200:203], a[148:151], a[68:71]\n\t"
      "v_mfma_f32_16x16x32_bf16 a[68:71], v[236:239], a[152:155], a[68:71]\n\t"
      "v_mfma_f32_16x16x32_bf16 v[48:51], v[200:203], a[152:155], v[48:51]\n\t"
      "ds_read_b128 v[96:99], v11 offset:256\n\t"
      "v_mfma_f32_16x16x32_bf16 v[48:51], v[236:239], a[156:159], v[48:51]\n\t"
      "v_mfma_f32_16x16x32_bf16 a[20:23], v[200:203], a[156:159], a[20:23]\n\t"
      "v_mfma_f32_16x16x32_bf16 a[20:23], v[236:239], a[160:163], a[20:23]\n\t"
      "v_mfma_f32_16x16x32_bf16 a[52:55], v[200:203], a[160:163], a[52:55]\n\t"
      "ds_read_b128 v[100:103], v12 offset:256\n\t"
      "v_mfma_f32_16x16x32_bf16 a[52:55], v[236:239], a[164:167], a[52:55]\n\t"
      "v_mfma_f32_16x16x32_bf16 a[84:87], v[200:203], a[164:167], a[84:87]\n\t"
      "ds_read_b64_tr_b16 v[168:169], v6 offset:9216\n\t"
      "v_mfma_f32_16x16x32_bf16 a[84:87], v[236:239], a[168:171], a[84:87]\n\t"
      "ds_read_b64_tr_b16 v[170:171], v6 offset:9728\n\t"
      "v_mfma_f32_16x16x32_bf16 v[64:67], v[200:203], a[168:171], v[64:67]\n\t"
      "ds_read_b64_tr_b16 v[244:245], v7 offset:9216\n\t"
      "v_mfma_f32_16x16x32_bf16 v[64:67], v[236:239], a[172:175], v[64:67]\n\t"
      "ds_read_b64_tr_b16 v[246:247], v7 offset:9728\n\t"
      "v_mfma_f32_16x16x32_bf16 a[8:11], v[176:179], a[140:143], a[8:11]\n\t"
      "ds_read_b64_tr_b16 v[172:173], v8 offset:9216\n\t"
      "v_mfma_f32_16x16x32_bf16 a[8:11], v[220:223], a[144:147], a[8:11]\n\t"
      "ds_read_b64_tr_b16 v[174:175], v8 offset:9728\n\t"
      "v_mfma_f32_16x16x32_bf16 a[40:43], v[176:179], a[144:147], a[40:43]\n\t"
      "ds_read_b64_tr_b16 v[164:165], v9 offset:9216\n\t"
      "v_mfma_f32_16x16x32_bf16 a[40:43], v[220:223], a[148:151], a[40:43]\n\t"
      "ds_read_b64_tr_b16 v[166:167], v9 offset:9728\n\t"
      "v_mfma_f32_16x16x32_bf16 a[72:75], v[176:179], a[148:151], a[72:75]\n\t"
      "ds_read_b128 v[104:107], v13 offset:256\n\t"
      "v_mfma_f32_16x16x32_bf16 a[72:75], v[220:223], a[152:155], a[72:75]\n\t"
      "v_mfma_f32_16x16x32_bf16 v[52:55], v[176:179], a[152:155], v[52:55]\n\t"
      "v_mfma_f32_16x16x32_bf16 v[52:55], v[220:223], a[156:159], v[52:55]\n\t"
      "v_mfma_f32_16x16x32_bf16 a[24:27], v[176:179], a[156:159], a[24:27]\n\t"
      "ds_read_b128 v[108:111], v14 offset:0\n\t"
      "v_mfma_f32_16x16x32_bf16 a[24:27], v[220:223], a[160:163], a[24:27]\n\t"
      "v_mfma_f32_16x16x32_bf16 a[56:59], v[176:179], a[160:163], a[56:59]\n\t"
      "v_mfma_f32_16x16x32_bf16 a[56:59], v[220:223], a[164:167], a[56:59]\n\t"
      "v_mfma_f32_16x16x32_bf16 a[88:91], v[176:179], a[164:167], a[88:91]\n\t"
      "ds_read_b128 v[112:115], v15 offset:0\n\t"
      "v_mfma_f32_16x16x32_bf16 a[88:91], v[220:223], a[168:171], a[88:91]\n\t"
      "v_mfma_f32_16x16x32_bf16 v[68:71], v[176:179], a[168:171], v[68:71]\n\t"
      "v_mfma_f32_16x16x32_bf16 v[68:71], v[220:223], a[172:175], v[68:71]\n\t"
      "v_mfma_f32_16x16x32_bf16 a[12:15], v[228:231], a[140:143], a[12:15]\n\t"
      "ds_read_b128 v[116:119], v16 offset:0\n\t"
      "v_mfma_f32_16x16x32_bf16 a[12:15], v[224:227], a[144:147], a[12:15]\n\t"
      "v_mfma_f32_16x16x32_bf16 a[44:47], v[228:231], a[144:147], a[44:47]\n\t"
      "ds_read_b64_tr_b16 v[180:181], v6 offset:13312\n\t"
      "v_mfma_f32_16x16x32_bf16 a[44:47], v[224:227], a[148:151], a[44:47]\n\t"
      "ds_read_b64_tr_b16 v[182:183], v6 offset:13824\n\t"
      "v_mfma_f32_16x16x32_bf16 a[76:79], v[228:231], a[148:151], a[76:79]\n\t"
      "ds_read_b64_tr_b16 v[232:233], v7 offset:13312\n\t"
      "v_mfma_f32_16x16x32_bf16 a[76:79], v[224:227], a[152:155], a[76:79]\n\t"
      "ds_read_b64_tr_b16 v[234:235], v7 offset:13824\n\t"
      "v_mfma_f32_16x16x32_bf16 v[56:59], v[228:231], a[152:155], v[56:59]\n\t"
      "ds_read_b64_tr_b16 v[208:209], v8 offset:13312\n\t"
      "v_mfma_f32_16x16x32_bf16 v[56:59], v[224:227], a[156:159], v[56:59]\n\t"
      "ds_read_b64_tr_b16 v[210:211], v8 offset:13824\n\t"
      "v_mfma_f32_16x16x32_bf16 a[28:31], v[228:231], a[156:159], a[28:31]\n\t"
      "ds_read_b64_tr_b16 v[252:253], v9 offset:13312\n\t"
      "v_mfma_f32_16x16x32_bf16 a[28:31], v[224:227], a[160:163], a[28:31]\n\t"
      "ds_read_b64_tr_b16 v[254:255], v9 offset:13824\n\t"
      "v_mfma_f32_16x16x32_bf16 a[60:63], v[228:231], a[160:163], a[60:63]\n\t"
      "ds_read_b128 v[120:123], v17 offset:0\n\t"
      "v_mfma_f32_16x16x32_bf16 a[60:63], v[224:227], a[164:167], a[60:63]\n\t"
      "v_mfma_f32_16x16x32_bf16 a[92:95], v[228:231], a[164:167], a[92:95]\n\t"
      "v_mfma_f32_16x16x32_bf16 a[92:95], v[224:227], a[168:171], a[92:95]\n\t"
      "v_mfma_f32_16x16x32_bf16 v[72:75], v[228:231], a[168:171], v[72:75]\n\t"
      "ds_read_b128 v[124:127], v14 offset:256\n\t"
      "v_mfma_f32_16x16x32_bf16 v[72:75], v[224:227], a[172:175], v[72:75]\n\t"
      "s_waitcnt lgkmcnt(1)\n\t"
      "v_mfma_f32_16x16x32_bf16 a[0:3], v[168:171], a[148:151], a[0:3]\n\t"
      "v_mfma_f32_16x16x32_bf16 a[0:3], v[180:183], a[152:155], a[0:3]\n\t"
      "v_mfma_f32_16x16x32_bf16 a[32:35], v[168:171], a[152:155], a[32:35]\n\t"
      "ds_read_b128 v[128:131], v15 offset:256\n\t"
      "v_mfma_f32_16x16x32_bf16 a[32:35], v[180:183], a[156:159], a[32:35]\n\t"
      "v_mfma_f32_16x16x32_bf16 a[64:67], v[168:171], a[156:159], a[64:67]\n\t"
      "v_mfma_f32_16x16x32_bf16 a[64:67], v[180:183], a[160:163], a[64:67]\n\t"
      "v_mfma_f32_16x16x32_bf16 v[44:47], v[168:171], a[160:163], v[44:47]\n\t"
      "ds_read_b128 v[132:135], v16 offset:256\n\t"
      "v_mfma_f32_16x16x32_bf16 v[44:47], v[180:183], a[164:167], v[44:47]\n\t"
      "v_mfma_f32_16x16x32_bf16 a[16:19], v[168:171], a[164:167], a[16:19]\n\t"
      "s_waitcnt vmcnt(12)\n\t"
      "ds_read_b64_tr_b16 v[204:205], v6 offset:17408\n\t"
      "v_mfma_f32_16x16x32_bf16 a[16:19], v[180:183], a[168:171], a[16:19]\n\t"
      "ds_read_b64_tr_b16 v[206:207], v6 offset:17920\n\t"
      "v_mfma_f32_16x16x32_bf16 a[48:51], v[168:171], a[168:171], a[48:51]\n\t"
      "ds_read_b64_tr_b16 v[184:185], v7 offset:17408\n\t"
      "v_mfma_f32_16x16x32_bf16 a[48:51], v[180:183], a[172:175], a[48:51]\n\t"
      "ds_read_b64_tr_b16 v[186:187], v7 offset:17920\n\t"
      "v_mfma_f32_16x16x32_bf16 a[80:83], v[168:171], a[172:175], a[80:83]\n\t"
      "ds_read_b64_tr_b16 v[192:193], v8 offset:17408\n\t"
      "v_mfma_f32_16x16x32_bf16 a[80:83], v[180:183], a[176:179], a[80:83]\n\t"
      "ds_read_b64_tr_b16 v[194:195], v8 offset:17920\n\t"
      "v_mfma_f32_16x16x32_bf16 v[60:63], v[168:171], a[176:179], v[60:63]\n\t"
      "ds_read_b64_tr_b16 v[196:197], v9 offset:17408\n\t"
      "v_mfma_f32_16x16x32_bf16 v[60:63], v[180:183], a[180:183], v[60:63]\n\t"
      "ds_read_b64_tr_b16 v[198:199], v9 offset:17920\n\t"
      "v_mfma_f32_16x16x32_bf16 a[4:7], v[244:247], a[148:151], a[4:7]\n\t"
      "ds_read_b128 v[136:139], v17 offset:256\n\t"
      "v_mfma_f32_16x16x32_bf16 a[4:7], v[232:235], a[152:155], a[4:7]\n\t"
      "v_mfma_f32_16x16x32_bf16 a[36:39], v[244:247], a[152:155], a[36:39]\n\t"
      "v_mfma_f32_16x16x32_bf16 a[36:39], v[232:235], a[156:159], a[36:39]\n\t"
      "v_mfma_f32_16x16x32_bf16 a[68:71], v[244:247], a[156:159], a[68:71]\n\t"
      "ds_read_b128 v[140:143], v18 offset:0\n\t"
      "v_mfma_f32_16x16x32_bf16 a[68:71], v[232:235], a[160:163], a[68:71]\n\t"
      "v_mfma_f32_16x16x32_bf16 v[48:51], v[244:247], a[160:163], v[48:51]\n\t"
      "v_mfma_f32_16x16x32_bf16 v[48:51], v[232:235], a[164:167], v[48:51]\n\t"
      "v_mfma_f32_16x16x32_bf16 a[20:23], v[244:247], a[164:167], a[20:23]\n\t"
      "ds_read_b128 v[144:147], v19 offset:0\n\t"
      "v_mfma_f32_16x16x32_bf16 a[20:23], v[232:235], a[168:171], a[20:23]\n\t"
      "v_mfma_f32_16x16x32_bf16 a[52:55], v[244:247], a[168:171], a[52:55]\n\t"
      "v_mfma_f32_16x16x32_bf16 a[52:55], v[232:235], a[172:175], a[52:55]\n\t"
      "v_mfma_f32_16x16x32_bf16 a[84:87], v[244:247], a[172:175], a[84:87]\n\t"
      "ds_read_b128 v[148:151], v20 offset:0\n\t"
      "v_mfma_f32_16x16x32_bf16 a[84:87], v[232:235], a[176:179], a[84:87]\n\t"
      "v_mfma_f32_16x16x32_bf16 v[64:67], v[244:247], a[176:179], v[64:67]\n\t"
      "s_waitcnt vmcnt(8)\n\t"
      "ds_read_b64_tr_b16 v[248:249], v6 offset:21504\n\t"
      "buffer_load_dwordx4 v3, s[20:23], s40 offen offset:1024 lds\n\t"
      "v_mfma_f32_16x16x32_bf16 v[64:67], v[232:235], a[180:183], v[64:67]\n\t"
      "ds_read_b64_tr_b16 v[250:251], v6 offset:22016\n\t"
      "v_mfma_f32_16x16x32_bf16 a[8:11], v[172:175], a[148:151], a[8:11]\n\t"
      "ds_read_b64_tr_b16 v[188:189], v7 offset:21504\n\t"
      "v_mfma_f32_16x16x32_bf16 a[8:11], v[208:211], a[152:155], a[8:11]\n\t"
      "ds_read_b64_tr_b16 v[190:191], v7 offset:22016\n\t"
      "v_mfma_f32_16x16x32_bf16 a[40:43], v[172:175], a[152:155], a[40:43]\n\t"
      "ds_read_b64_tr_b16 v[212:213], v8 offset:21504\n\t"
      "buffer_load_dwordx4 v4, s[20:23], s40 offen offset:2048 lds\n\t"
      "v_mfma_f32_16x16x32_bf16 a[40:43], v[208:211], a[156:159], a[40:43]\n\t"
      "ds_read_b64_tr_b16 v[214:215], v8 offset:22016\n\t"
      "v_mfma_f32_16x16x32_bf16 a[72:75], v[172:175], a[156:159], a[72:75]\n\t"
      "ds_read_b64_tr_b16 v[240:241], v9 offset:21504\n\t"
      "v_mfma_f32_16x16x32_bf16 a[72:75], v[208:211], a[160:163], a[72:75]\n\t"
      "ds_read_b64_tr_b16 v[242:243], v9 offset:22016\n\t"
      "v_mfma_f32_16x16x32_bf16 v[52:55], v[172:175], a[160:163], v[52:55]\n\t"
      "ds_read_b128 v[152:155], v21 offset:0\n\t"
      "buffer_load_dwordx4 v5, s[20:23], s40 offen offset:3072 lds\n\t"
      "v_mfma_f32_16x16x32_bf16 v[52:55], v[208:211], a[164:167], v[52:55]\n\t"
      "v_mfma_f32_16x16x32_bf16 a[24:27], v[172:175], a[164:167], a[24:27]\n\t"
      "v_mfma_f32_16x16x32_bf16 a[24:27], v[208:211], a[168:171], a[24:27]\n\t"
      "s_add_u32 m0, s37, 33792\n\t"
      "s_mov_b32 s40, 4718592\n\t"
      "v_mfma_f32_16x16x32_bf16 a[56:59], v[172:175], a[168:171], a[56:59]\n\t"
      "ds_read_b128 v[156:159], v18 offset:256\n\t"
      "buffer_load_dwordx4 v2, s[20:23], s40 offen offset:0 lds\n\t"
      "v_mfma_f32_16x16x32_bf16 a[56:59], v[208:211], a[172:175], a[56:59]\n\t"
      "v_mfma_f32_16x16x32_bf16 a[88:91], v[172:175], a[172:175], a[88:91]\n\t"
      "v_mfma_f32_16x16x32_bf16 a[88:91], v[208:211], a[176:179], a[88:91]\n\t"
      "v_mfma_f32_16x16x32_bf16 v[68:71], v[172:175], a[176:179], v[68:71]\n\t"
      "ds_read_b128 v[160:163], v19 offset:256\n\t"
      "buffer_load_dwordx4 v3, s[20:23], s40 offen offset:1024 lds\n\t"
      "v_mfma_f32_16x16x32_bf16 v[68:71], v[208:211], a[180:183], v[68:71]\n\t"
      "v_mfma_f32_16x16x32_bf16 a[12:15], v[164:167], a[148:151], a[12:15]\n\t"
      "v_mfma_f32_16x16x32_bf16 a[12:15], v[252:255], a[152:155], a[12:15]\n\t"
      "v_mfma_f32_16x16x32_bf16 a[44:47], v[164:167], a[152:155], a[44:47]\n\t"
      "buffer_load_dwordx4 v4, s[20:23], s40 offen offset:2048 lds\n\t"
      "v_mfma_f32_16x16x32_bf16 a[44:47], v[252:255], a[156:159], a[44:47]\n\t"
      "v_mfma_f32_16x16x32_bf16 a[76:79], v[164:167], a[156:159], a[76:79]\n\t"
      "s_waitcnt vmcnt(10)\n\t"
      "ds_read_b64_tr_b16 v[216:217], v6 offset:25600\n\t"
      "v_mfma_f32_16x16x32_bf16 a[76:79], v[252:255], a[160:163], a[76:79]\n\t"
      "ds_read_b64_tr_b16 v[218:219], v6 offset:26112\n\t"
      "v_mfma_f32_16x16x32_bf16 v[56:59], v[164:167], a[160:163], v[56:59]\n\t"
      "ds_read_b64_tr_b16 v[200:201], v7 offset:25600\n\t"
      "buffer_load_dwordx4 v5, s[20:23], s40 offen offset:3072 lds\n\t"
      "v_mfma_f32_16x16x32_bf16 v[56:59], v[252:255], a[164:167], v[56:59]\n\t"
      "ds_read_b64_tr_b16 v[202:203], v7 offset:26112\n\t"
      "v_mfma_f32_16x16x32_bf16 a[28:31], v[164:167], a[164:167], a[28:31]\n\t"
      "ds_read_b64_tr_b16 v[236:237], v8 offset:25600\n\t"
      "v_mfma_f32_16x16x32_bf16 a[28:31], v[252:255], a[168:171], a[28:31]\n\t"
      "ds_read_b64_tr_b16 v[238:239], v8 offset:26112\n\t"
      "s_add_u32 m0, s37, 9216\n\t"
      "s_mov_b32 s40, 5767168\n\t"
      "v_mfma_f32_16x16x32_bf16 a[60:63], v[164:167], a[168:171], a[60:63]\n\t"
      "ds_read_b64_tr_b16 v[176:177], v9 offset:25600\n\t"
      "buffer_load_dwordx4 v2, s[20:23], s40 offen offset:0 lds\n\t"
      "v_mfma_f32_16x16x32_bf16 a[60:63], v[252:255], a[172:175], a[60:63]\n\t"
      "ds_read_b64_tr_b16 v[178:179], v9 offset:26112\n\t"
      "v_mfma_f32_16x16x32_bf16 a[92:95], v[164:167], a[172:175], a[92:95]\n\t"
      "v_mfma_f32_16x16x32_bf16 a[92:95], v[252:255], a[176:179], a[92:95]\n\t"
      "v_mfma_f32_16x16x32_bf16 v[72:75], v[164:167], a[176:179], v[72:75]\n\t"
      "buffer_load_dwordx4 v3, s[20:23], s40 offen offset:1024 lds\n\t"
      "v_mfma_f32_16x16x32_bf16 v[72:75], v[252:255], a[180:183], v[72:75]\n\t"
      "s_waitcnt lgkmcnt(1)\n\t"
      "v_mfma_f32_16x16x32_bf16 a[0:3], v[204:207], v[76:79], a[0:3]\n\t"
      "v_mfma_f32_16x16x32_bf16 a[0:3], v[248:251], v[80:83], a[0:3]\n\t"
      "v_mfma_f32_16x16x32_bf16 a[0:3], v[216:219], v[84:87], a[0:3]\n\t"
      "buffer_load_dwordx4 v4, s[20:23], s40 offen offset:2048 lds\n\t"
      "v_mfma_f32_16x16x32_bf16 a[32:35], v[204:207], v[80:83], a[32:35]\n\t"
      "v_mfma_f32_16x16x32_bf16 a[32:35], v[248:251], v[84:87], a[32:35]\n\t"
      "v_mfma_f32_16x16x32_bf16 a[32:35], v[216:219], v[88:91], a[32:35]\n\t"
      "v_mfma_f32_16x16x32_bf16 a[64:67], v[204:207], v[84:87], a[64:67]\n\t"
      "buffer_load_dwordx4 v5, s[20:23], s40 offen offset:3072 lds\n\t"
      "v_mfma_f32_16x16x32_bf16 a[64:67], v[248:251], v[88:91], a[64:67]\n\t"
      "v_mfma_f32_16x16x32_bf16 a[64:67], v[216:219], v[92:95], a[64:67]\n\t"
      "v_mfma_f32_16x16x32_bf16 v[44:47], v[204:207], v[88:91], v[44:47]\n\t"
      "s_add_u32 m0, s37, 13312\n\t"
      "s_mov_b32 s40, 6815744\n\t"
      "v_mfma_f32_16x16x32_bf16 v[44:47], v[248:251], v[92:95], v[44:47]\n\t"
      "buffer_load_dwordx4 v2, s[20:23], s40 offen offset:0 lds\n\t"
      "v_mfma_f32_16x16x32_bf16 v[44:47], v[216:219], v[96:99], v[44:47]\n\t"
      "v_mfma_f32_16x16x32_bf16 a[16:19], v[204:207], v[92:95], a[16:19]\n\t"
      "v_mfma_f32_16x16x32_bf16 a[16:19], v[248:251], v[96:99], a[16:19]\n\t"
      "v_mfma_f32_16x16x32_bf16 a[16:19], v[216:219], v[100:103], a[16:19]\n\t"
      "buffer_load_dwordx4 v3, s[20:23], s40 offen offset:1024 lds\n\t"
      "v_mfma_f32_16x16x32_bf16 a[48:51], v[204:207], v[96:99], a[48:51]\n\t"
      "v_mfma_f32_16x16x32_bf16 a[48:51], v[248:251], v[100:103], a[48:51]\n\t"
      "v_mfma_f32_16x16x32_bf16 a[48:51], v[216:219], v[104:107], a[48:51]\n\t"
      "v_mfma_f32_16x16x32_bf16 a[80:83], v[204:207], v[100:103], a[80:83]\n\t"
      "buffer_load_dwordx4 v4, s[20:23], s40 offen offset:2048 lds\n\t"
      "v_mfma_f32_16x16x32_bf16 a[80:83], v[248:251], v[104:107], a[80:83]\n\t"
      "v_mfma_f32_16x16x32_bf16 a[80:83], v[216:219], v[108:111], a[80:83]\n\t"
      "v_mfma_f32_16x16x32_bf16 v[60:63], v[204:207], v[104:107], v[60:63]\n\t"
      "v_mfma_f32_16x16x32_bf16 v[60:63], v[248:251], v[108:111], v[60:63]\n\t"
      "buffer_load_dwordx4 v5, s[20:23], s40 offen offset:3072 lds\n\t"
      "v_mfma_f32_16x16x32_bf16 v[60:63], v[216:219], v[112:115], v[60:63]\n\t"
      "v_mfma_f32_16x16x32_bf16 a[4:7], v[184:187], v[76:79], a[4:7]\n\t"
      "v_mfma_f32_16x16x32_bf16 a[4:7], v[188:191], v[80:83], a[4:7]\n\t"
      "s_add_u32 m0, s37, 17408\n\t"
      "s_mov_b32 s40, 7864320\n\t"
      "v_mfma_f32_16x16x32_bf16 a[4:7], v[200:203], v[84:87], a[4:7]\n\t"
      "buffer_load_dwordx4 v2, s[20:23], s40 offen offset:0 lds\n\t"
      "v_mfma_f32_16x16x32_bf16 a[36:39], v[184:187], v[80:83], a[36:39]\n\t"
      "v_mfma_f32_16x16x32_bf16 a[36:39], v[188:191], v[84:87], a[36:39]\n\t"
      "v_mfma_f32_16x16x32_bf16 a[36:39], v[200:203], v[88:91], a[36:39]\n\t"
      "v_mfma_f32_16x16x32_bf16 a[68:71], v[184:187], v[84:87], a[68:71]\n\t"
      "buffer_load_dwordx4 v3, s[20:23], s40 offen offset:1024 lds\n\t"
      "v_mfma_f32_16x16x32_bf16 a[68:71], v[188:191], v[88:91], a[68:71]\n\t"
      "v_mfma_f32_16x16x32_bf16 a[68:71], v[200:203], v[92:95], a[68:71]\n\t"
      "v_mfma_f32_16x16x32_bf16 v[48:51], v[184:187], v[88:91], v[48:51]\n\t"
      "v_mfma_f32_16x16x32_bf16 v[48:51], v[188:191], v[92:95], v[48:51]\n\t"
      "buffer_load_dwordx4 v4, s[20:23], s40 offen offset:2048 lds\n\t"
      "v_mfma_f32_16x16x32_bf16 v[48:51], v[200:203], v[96:99], v[48:51]\n\t"
      "v_mfma_f32_16x16x32_bf16 a[20:23], v[184:187], v[92:95], a[20:23]\n\t"
      "v_mfma_f32_16x16x32_bf16 a[20:23], v[188:191], v[96:99], a[20:23]\n\t"
      "v_mfma_f32_16x16x32_bf16 a[20:23], v[200:203], v[100:103], a[20:23]\n\t"
      "buffer_load_dwordx4 v5, s[20:23], s40 offen offset:3072 lds\n\t"
      "v_mfma_f32_16x16x32_bf16 a[52:55], v[184:187], v[96:99], a[52:55]\n\t"
      "v_mfma_f32_16x16x32_bf16 a[52:55], v[188:191], v[100:103], a[52:55]\n\t"
      "v_mfma_f32_16x16x32_bf16 a[52:55], v[200:203], v[104:107], a[52:55]\n\t"
      "s_add_u32 m0, s37, 21504\n\t"
      "s_mov_b32 s40, 8912896\n\t"
      "v_mfma_f32_16x16x32_bf16 a[84:87], v[184:187], v[100:103], a[84:87]\n\t"
      "buffer_load_dwordx4 v2, s[20:23], s40 offen offset:0 lds\n\t"
      "v_mfma_f32_16x16x32_bf16 a[84:87], v[188:191], v[104:107], a[84:87]\n\t"
      "v_mfma_f32_16x16x32_bf16 a[84:87], v[200:203], v[108:111], a[84:87]\n\t"
      "v_mfma_f32_16x16x32_bf16 v[64:67], v[184:187], v[104:107], v[64:67]\n\t"
      "v_mfma_f32_16x16x32_bf16 v[64:67], v[188:191], v[108:111], v[64:67]\n\t"
      "buffer_load_dwordx4 v3, s[20:23], s40 offen offset:1024 lds\n\t"
      "v_mfma_f32_16x16x32_bf16 v[64:67], v[200:203], v[112:115], v[64:67]\n\t"
      "v_mfma_f32_16x16x32_bf16 a[8:11], v[192:195], v[76:79], a[8:11]\n\t"
      "v_mfma_f32_16x16x32_bf16 a[8:11], v[212:215], v[80:83], a[8:11]\n\t"
      "v_mfma_f32_16x16x32_bf16 a[8:11], v[236:239], v[84:87], a[8:11]\n\t"
      "buffer_load_dwordx4 v4, s[20:23], s40 offen offset:2048 lds\n\t"
      "v_mfma_f32_16x16x32_bf16 a[40:43], v[192:195], v[80:83], a[40:43]\n\t"
      "v_mfma_f32_16x16x32_bf16 a[40:43], v[212:215], v[84:87], a[40:43]\n\t"
      "v_mfma_f32_16x16x32_bf16 a[40:43], v[236:239], v[88:91], a[40:43]\n\t"
      "v_mfma_f32_16x16x32_bf16 a[72:75], v[192:195], v[84:87], a[72:75]\n\t"
      "buffer_load_dwordx4 v5, s[20:23], s40 offen offset:3072 lds\n\t"
      "v_mfma_f32_16x16x32_bf16 a[72:75], v[212:215], v[88:91], a[72:75]\n\t"
      "v_mfma_f32_16x16x32_bf16 a[72:75], v[236:239], v[92:95], a[72:75]\n\t"
      "v_mfma_f32_16x16x32_bf16 v[52:55], v[192:195], v[88:91], v[52:55]\n\t"
      "s_add_u32 m0, s37, 25600\n\t"
      "s_mov_b32 s40, 9961472\n\t"
      "v_mfma_f32_16x16x32_bf16 v[52:55], v[212:215], v[92:95], v[52:55]\n\t"
      "s_waitcnt lgkmcnt(0)\n\t"
      "buffer_load_dwordx4 v2, s[20:23], s40 offen offset:0 lds\n\t"
      "v_mfma_f32_16x16x32_bf16 v[52:55], v[236:239], v[96:99], v[52:55]\n\t"
      "v_mfma_f32_16x16x32_bf16 a[24:27], v[192:195], v[92:95], a[24:27]\n\t"
      "v_mfma_f32_16x16x32_bf16 a[24:27], v[212:215], v[96:99], a[24:27]\n\t"
      "s_waitcnt vmcnt(21)\n\t"
      "ds_read_b64_tr_b16 v[220:221], v6 offset:29696\n\t"
      "v_mfma_f32_16x16x32_bf16 a[24:27], v[236:239], v[100:103], a[24:27]\n\t"
      "ds_read_b64_tr_b16 v[222:223], v6 offset:30208\n\t"
      "buffer_load_dwordx4 v3, s[20:23], s40 offen offset:1024 lds\n\t"
      "v_mfma_f32_16x16x32_bf16 a[56:59], v[192:195], v[96:99], a[56:59]\n\t"
      "ds_read_b64_tr_b16 v[228:229], v7 offset:29696\n\t"
      "v_mfma_f32_16x16x32_bf16 a[56:59], v[212:215], v[100:103], a[56:59]\n\t"
      "ds_read_b64_tr_b16 v[230:231], v7 offset:30208\n\t"
      "v_mfma_f32_16x16x32_bf16 a[56:59], v[236:239], v[104:107], a[56:59]\n\t"
      "ds_read_b64_tr_b16 v[224:225], v8 offset:29696\n\t"
      "v_mfma_f32_16x16x32_bf16 a[88:91], v[192:195], v[100:103], a[88:91]\n\t"
      "ds_read_b64_tr_b16 v[226:227], v8 offset:30208\n\t"
      "buffer_load_dwordx4 v4, s[20:23], s40 offen offset:2048 lds\n\t"
      "v_mfma_f32_16x16x32_bf16 a[88:91], v[212:215], v[104:107], a[88:91]\n\t"
      "ds_read_b64_tr_b16 v[168:169], v9 offset:29696\n\t"
      "v_mfma_f32_16x16x32_bf16 a[88:91], v[236:239], v[108:111], a[88:91]\n\t"
      "ds_read_b64_tr_b16 v[170:171], v9 offset:30208\n\t"
      "v_mfma_f32_16x16x32_bf16 v[68:71], v[192:195], v[104:107], v[68:71]\n\t"
      "v_mfma_f32_16x16x32_bf16 v[68:71], v[212:215], v[108:111], v[68:71]\n\t"
      "buffer_load_dwordx4 v5, s[20:23], s40 offen offset:3072 lds\n\t"
      "v_mfma_f32_16x16x32_bf16 v[68:71], v[236:239], v[112:115], v[68:71]\n\t"
      "v_mfma_f32_16x16x32_bf16 a[12:15], v[196:199], v[76:79], a[12:15]\n\t"
      "v_mfma_f32_16x16x32_bf16 a[12:15], v[240:243], v[80:83], a[12:15]\n\t"
      "s_add_u32 m0, s37, 29696\n\t"
      "s_mov_b32 s40, 11010048\n\t"
      "v_mfma_f32_16x16x32_bf16 a[12:15], v[176:179], v[84:87], a[12:15]\n\t"
      "s_waitcnt lgkmcnt(0)\n\t"
      "buffer_load_dwordx4 v2, s[20:23], s40 offen offset:0 lds\n\t"
      "v_mfma_f32_16x16x32_bf16 a[44:47], v[196:199], v[80:83], a[44:47]\n\t"
      "v_mfma_f32_16x16x32_bf16 a[44:47], v[240:243], v[84:87], a[44:47]\n\t"
      "v_mfma_f32_16x16x32_bf16 a[44:47], v[176:179], v[88:91], a[44:47]\n\t"
      "v_mfma_f32_16x16x32_bf16 a[76:79], v[196:199], v[84:87], a[76:79]\n\t"
      "buffer_load_dwordx4 v3, s[20:23], s40 offen offset:1024 lds\n\t"
      "v_mfma_f32_16x16x32_bf16 a[76:79], v[240:243], v[88:91], a[76:79]\n\t"
      "v_mfma_f32_16x16x32_bf16 a[76:79], v[176:179], v[92:95], a[76:79]\n\t"
      "s_barrier\n\t"
      "v_mfma_f32_16x16x32_bf16 v[56:59], v[196:199], v[88:91], v[56:59]\n\t"
      "s_mov_b32 s57, 9175040\n\t"
      "buffer_load_dword v35, v33, s[20:23], s57 offen\n\t"
      "v_mfma_f32_16x16x32_bf16 v[56:59], v[240:243], v[92:95], v[56:59]\n\t"
      "s_mov_b32 m0, s59\n\t"
      "v_mfma_f32_16x16x32_bf16 v[56:59], v[176:179], v[96:99], v[56:59]\n\t"
      "s_waitcnt vmcnt(23)\n\t"
      "ds_read_b64_tr_b16 v[180:181], v6 offset:33792\n\t"
      "v_mfma_f32_16x16x32_bf16 a[28:31], v[196:199], v[92:95], a[28:31]\n\t"
      "ds_read_b64_tr_b16 v[182:183], v6 offset:34304\n\t"
      "v_mfma_f32_16x16x32_bf16 a[28:31], v[240:243], v[96:99], a[28:31]\n\t"
      "ds_read_b64_tr_b16 v[244:245], v7 offset:33792\n\t"
      "v_mfma_f32_16x16x32_bf16 a[28:31], v[176:179], v[100:103], a[28:31]\n\t"
      "ds_read_b64_tr_b16 v[246:247], v7 offset:34304\n\t"
      "v_mfma_f32_16x16x32_bf16 a[60:63], v[196:199], v[96:99], a[60:63]\n\t"
      "ds_read_b64_tr_b16 v[232:233], v8 offset:33792\n\t"
      "v_mfma_f32_16x16x32_bf16 a[60:63], v[240:243], v[100:103], a[60:63]\n\t"
      "ds_read_b64_tr_b16 v[234:235], v8 offset:34304\n\t"
      "v_mfma_f32_16x16x32_bf16 a[60:63], v[176:179], v[104:107], a[60:63]\n\t"
      "ds_read_b64_tr_b16 v[172:173], v9 offset:33792\n\t"
      "v_mfma_f32_16x16x32_bf16 a[92:95], v[196:199], v[100:103], a[92:95]\n\t"
      "ds_read_b64_tr_b16 v[174:175], v9 offset:34304\n\t"
      "v_mfma_f32_16x16x32_bf16 a[92:95], v[240:243], v[104:107], a[92:95]\n\t"
      "buffer_load_dwordx4 v22, s[16:19], s54 offen offset:0 lds\n\t"
      "v_mfma_f32_16x16x32_bf16 a[92:95], v[176:179], v[108:111], a[92:95]\n\t"
      "v_mfma_f32_16x16x32_bf16 v[72:75], v[196:199], v[104:107], v[72:75]\n\t"
      "v_mfma_f32_16x16x32_bf16 v[72:75], v[240:243], v[108:111], v[72:75]\n\t"
      "s_mov_b32 m0, s100\n\t"
      "v_mfma_f32_16x16x32_bf16 v[72:75], v[176:179], v[112:115], v[72:75]\n\t"
      "buffer_load_dwordx4 v23, s[16:19], s54 offen offset:0 lds\n\t"
      "v_mfma_f32_16x16x32_bf16 a[0:3], v[220:223], v[88:91], a[0:3]\n\t"
      "s_waitcnt lgkmcnt(0)\n\t"
      "v_mfma_f32_16x16x32_bf16 a[0:3], v[180:183], v[92:95], a[0:3]\n\t"
      "v_mfma_f32_16x16x32_bf16 a[32:35], v[220:223], v[92:95], a[32:35]\n\t"
      "v_mfma_f32_16x16x32_bf16 a[32:35], v[180:183], v[96:99], a[32:35]\n\t"
      "buffer_load_dwordx4 v24, s[16:19], s54 offen offset:1024 lds\n\t"
      "v_mfma_f32_16x16x32_bf16 a[64:67], v[220:223], v[96:99], a[64:67]\n\t"
      "v_mfma_f32_16x16x32_bf16 a[64:67], v[180:183], v[100:103], a[64:67]\n\t"
      "v_mfma_f32_16x16x32_bf16 v[44:47], v[220:223], v[100:103], v[44:47]\n\t"
      "v_mfma_f32_16x16x32_bf16 v[44:47], v[180:183], v[104:107], v[44:47]\n\t"
      "buffer_load_dwordx4 v25, s[16:19], s54 offen offset:2048 lds\n\t"
      "v_mfma_f32_16x16x32_bf16 a[16:19], v[220:223], v[104:107], a[16:19]\n\t"
      "v_mfma_f32_16x16x32_bf16 a[16:19], v[180:183], v[108:111], a[16:19]\n\t"
      "v_mfma_f32_16x16x32_bf16 a[48:51], v[220:223], v[108:111], a[48:51]\n\t"
      "s_add_u32 m0, s37, 8192\n\t"
      "v_mfma_f32_16x16x32_bf16 a[48:51], v[180:183], v[112:115], a[48:51]\n\t"
      "buffer_load_dwordx4 v26, s[16:19], s54 offen offset:0 lds\n\t"
      "v_mfma_f32_16x16x32_bf16 a[80:83], v[220:223], v[112:115], a[80:83]\n\t"
      "s_waitcnt vmcnt(24)\n\t"
      "ds_read_b64_tr_b16 v[208:209], v6 offset:9216\n\t"
      "v_mfma_f32_16x16x32_bf16 a[80:83], v[180:183], v[116:119], a[80:83]\n\t"
      "ds_read_b64_tr_b16 v[210:211], v6 offset:9728\n\t"
      "v_mfma_f32_16x16x32_bf16 v[60:63], v[220:223], v[116:119], v[60:63]\n\t"
      "ds_read_b64_tr_b16 v[164:165], v7 offset:9216\n\t"
      "v_mfma_f32_16x16x32_bf16 v[60:63], v[180:183], v[120:123], v[60:63]\n\t"
      "ds_read_b64_tr_b16 v[166:167], v7 offset:9728\n\t"
      "v_mfma_f32_16x16x32_bf16 a[4:7], v[228:231], v[88:91], a[4:7]\n\t"
      "ds_read_b64_tr_b16 v[252:253], v8 offset:9216\n\t"
      "v_mfma_f32_16x16x32_bf16 a[4:7], v[244:247], v[92:95], a[4:7]\n\t"
      "ds_read_b64_tr_b16 v[254:255], v8 offset:9728\n\t"
      "v_mfma_f32_16x16x32_bf16 a[36:39], v[228:231], v[92:95], a[36:39]\n\t"
      "ds_read_b64_tr_b16 v[204:205], v9 offset:9216\n\t"
      "v_mfma_f32_16x16x32_bf16 a[36:39], v[244:247], v[96:99], a[36:39]\n\t"
      "ds_read_b64_tr_b16 v[206:207], v9 offset:9728\n\t"
      "v_mfma_f32_16x16x32_bf16 a[68:71], v[228:231], v[96:99], a[68:71]\n\t"
      "s_mov_b64 exec, 1\n\t"
      "buffer_store_dword v36, off, s[28:31], s46 offset:4 sc1\n\t"
      "s_mov_b64 exec, -1\n\t"
      "v_mfma_f32_16x16x32_bf16 a[68:71], v[244:247], v[100:103], a[68:71]\n\t"
      "v_mfma_f32_16x16x32_bf16 v[48:51], v[228:231], v[100:103], v[48:51]\n\t"
      "v_mfma_f32_16x16x32_bf16 v[48:51], v[244:247], v[104:107], v[48:51]\n\t"
      "v_mfma_f32_16x16x32_bf16 a[20:23], v[228:231], v[104:107], a[20:23]\n\t"
      "v_mfma_f32_16x16x32_bf16 a[20:23], v[244:247], v[108:111], a[20:23]\n\t"
      "v_mfma_f32_16x16x32_bf16 a[52:55], v[228:231], v[108:111], a[52:55]\n\t"
      "v_mfma_f32_16x16x32_bf16 a[52:55], v[244:247], v[112:115], a[52:55]\n\t"
      "v_mfma_f32_16x16x32_bf16 a[84:87], v[228:231], v[112:115], a[84:87]\n\t"
      "v_mfma_f32_16x16x32_bf16 a[84:87], v[244:247], v[116:119], a[84:87]\n\t"
      "v_mfma_f32_16x16x32_bf16 v[64:67], v[228:231], v[116:119], v[64:67]\n\t"
      "v_mfma_f32_16x16x32_bf16 v[64:67], v[244:247], v[120:123], v[64:67]\n\t"
      "v_mfma_f32_16x16x32_bf16 a[8:11], v[224:227], v[88:91], a[8:11]\n\t"
      "v_mfma_f32_16x16x32_bf16 a[8:11], v[232:235], v[92:95], a[8:11]\n\t"
      "v_mfma_f32_16x16x32_bf16 a[40:43], v[224:227], v[92:95], a[40:43]\n\t"
      "s_waitcnt vmcnt(21)\n\t"
      "ds_read_b64_tr_b16 v[248:249], v6 offset:13312\n\t"
      "v_mfma_f32_16x16x32_bf16 a[40:43], v[232:235], v[96:99], a[40:43]\n\t"
      "ds_read_b64_tr_b16 v[250:251], v6 offset:13824\n\t"
      "v_mfma_f32_16x16x32_bf16 a[72:75], v[224:227], v[96:99], a[72:75]\n\t"
      "ds_read_b64_tr_b16 v[216:217], v7 offset:13312\n\t"
      "v_mfma_f32_16x16x32_bf16 a[72:75], v[232:235], v[100:103], a[72:75]\n\t"
      "ds_read_b64_tr_b16 v[218:219], v7 offset:13824\n\t"
      "v_mfma_f32_16x16x32_bf16 v[52:55], v[224:227], v[100:103], v[52:55]\n\t"
      "ds_read_b64_tr_b16 v[184:185], v8 offset:13312\n\t"
      "v_mfma_f32_16x16x32_bf16 v[52:55], v[232:235], v[104:107], v[52:55]\n\t"
      "ds_read_b64_tr_b16 v[186:187], v8 offset:13824\n\t"
      "v_mfma_f32_16x16x32_bf16 a[24:27], v[224:227], v[104:107], a[24:27]\n\t"
      "ds_read_b64_tr_b16 v[188:189], v9 offset:13312\n\t"
      "v_mfma_f32_16x16x32_bf16 a[24:27], v[232:235], v[108:111], a[24:27]\n\t"
      "ds_read_b64_tr_b16 v[190:191], v9 offset:13824\n\t"
      "v_mfma_f32_16x16x32_bf16 a[56:59], v[224:227], v[108:111], a[56:59]\n\t"
      "v_mfma_f32_16x16x32_bf16 a[56:59], v[232:235], v[112:115], a[56:59]\n\t"
      "v_mfma_f32_16x16x32_bf16 a[88:91], v[224:227], v[112:115], a[88:91]\n\t"
      "v_mfma_f32_16x16x32_bf16 a[88:91], v[232:235], v[116:119], a[88:91]\n\t"
      "v_mfma_f32_16x16x32_bf16 v[68:71], v[224:227], v[116:119], v[68:71]\n\t"
      "v_mfma_f32_16x16x32_bf16 v[68:71], v[232:235], v[120:123], v[68:71]\n\t"
      "v_mfma_f32_16x16x32_bf16 a[12:15], v[168:171], v[88:91], a[12:15]\n\t"
      "v_mfma_f32_16x16x32_bf16 a[12:15], v[172:175], v[92:95], a[12:15]\n\t"
      "v_mfma_f32_16x16x32_bf16 a[44:47], v[168:171], v[92:95], a[44:47]\n\t"
      "v_mfma_f32_16x16x32_bf16 a[44:47], v[172:175], v[96:99], a[44:47]\n\t"
      "v_mfma_f32_16x16x32_bf16 a[76:79], v[168:171], v[96:99], a[76:79]\n\t"
      "v_mfma_f32_16x16x32_bf16 a[76:79], v[172:175], v[100:103], a[76:79]\n\t"
      "v_mfma_f32_16x16x32_bf16 v[56:59], v[168:171], v[100:103], v[56:59]\n\t"
      "v_mfma_f32_16x16x32_bf16 v[56:59], v[172:175], v[104:107], v[56:59]\n\t"
      "v_mfma_f32_16x16x32_bf16 a[28:31], v[168:171], v[104:107], a[28:31]\n\t"
      "s_waitcnt vmcnt(17)\n\t"
      "ds_read_b64_tr_b16 v[200:201], v6 offset:17408\n\t"
      "v_mfma_f32_16x16x32_bf16 a[28:31], v[172:175], v[108:111], a[28:31]\n\t"
      "ds_read_b64_tr_b16 v[202:203], v6 offset:17920\n\t"
      "v_mfma_f32_16x16x32_bf16 a[60:63], v[168:171], v[108:111], a[60:63]\n\t"
      "ds_read_b64_tr_b16 v[192:193], v7 offset:17408\n\t"
      "v_mfma_f32_16x16x32_bf16 a[60:63], v[172:175], v[112:115], a[60:63]\n\t"
      "ds_read_b64_tr_b16 v[194:195], v7 offset:17920\n\t"
      "v_mfma_f32_16x16x32_bf16 a[92:95], v[168:171], v[112:115], a[92:95]\n\t"
      "ds_read_b64_tr_b16 v[212:213], v8 offset:17408\n\t"
      "v_mfma_f32_16x16x32_bf16 a[92:95], v[172:175], v[116:119], a[92:95]\n\t"
      "ds_read_b64_tr_b16 v[214:215], v8 offset:17920\n\t"
      "v_mfma_f32_16x16x32_bf16 v[72:75], v[168:171], v[116:119], v[72:75]\n\t"
      "ds_read_b64_tr_b16 v[236:237], v9 offset:17408\n\t"
      "v_mfma_f32_16x16x32_bf16 v[72:75], v[172:175], v[120:123], v[72:75]\n\t"
      "ds_read_b64_tr_b16 v[238:239], v9 offset:17920\n\t"
      "s_waitcnt lgkmcnt(5)\n\t"
      "v_mfma_f32_16x16x32_bf16 a[0:3], v[208:211], v[96:99], a[0:3]\n\t"
      "s_add_u32 m0, s37, 29696\n\t"
      "s_nop 0\n\t"
      "buffer_load_dwordx4 v4, s[20:23], s40 offen offset:2048 lds\n\t"
      "v_mfma_f32_16x16x32_bf16 a[0:3], v[248:251], v[100:103], a[0:3]\n\t"
      "v_mfma_f32_16x16x32_bf16 a[32:35], v[208:211], v[100:103], a[32:35]\n\t"
      "v_mfma_f32_16x16x32_bf16 a[32:35], v[248:251], v[104:107], a[32:35]\n\t"
      "v_mfma_f32_16x16x32_bf16 a[64:67], v[208:211], v[104:107], a[64:67]\n\t"
      "buffer_load_dwordx4 v5, s[20:23], s40 offen offset:3072 lds\n\t"
      "v_mfma_f32_16x16x32_bf16 a[64:67], v[248:251], v[108:111], a[64:67]\n\t"
      "v_mfma_f32_16x16x32_bf16 v[44:47], v[208:211], v[108:111], v[44:47]\n\t"
      "v_mfma_f32_16x16x32_bf16 v[44:47], v[248:251], v[112:115], v[44:47]\n\t"
      "s_add_u32 m0, s37, 33792\n\t"
      "s_mov_b32 s40, 12058624\n\t"
      "v_mfma_f32_16x16x32_bf16 a[16:19], v[208:211], v[112:115], a[16:19]\n\t"
      "buffer_load_dwordx4 v2, s[20:23], s40 offen offset:0 lds\n\t"
      "v_mfma_f32_16x16x32_bf16 a[16:19], v[248:251], v[116:119], a[16:19]\n\t"
      "v_mfma_f32_16x16x32_bf16 a[48:51], v[208:211], v[116:119], a[48:51]\n\t"
      "v_mfma_f32_16x16x32_bf16 a[48:51], v[248:251], v[120:123], a[48:51]\n\t"
      "v_mfma_f32_16x16x32_bf16 a[80:83], v[208:211], v[120:123], a[80:83]\n\t"
      "buffer_load_dwordx4 v3, s[20:23], s40 offen offset:1024 lds\n\t"
      "v_mfma_f32_16x16x32_bf16 a[80:83], v[248:251], v[124:127], a[80:83]\n\t"
      "v_mfma_f32_16x16x32_bf16 v[60:63], v[208:211], v[124:127], v[60:63]\n\t"
      "s_waitcnt vmcnt(17)\n\t"
      "ds_read_b64_tr_b16 v[196:197], v6 offset:21504\n\t"
      "v_mfma_f32_16x16x32_bf16 v[60:63], v[248:251], v[128:131], v[60:63]\n\t"
      "ds_read_b64_tr_b16 v[198:199], v6 offset:22016\n\t"
      "v_mfma_f32_16x16x32_bf16 a[4:7], v[164:167], v[96:99], a[4:7]\n\t"
      "ds_read_b64_tr_b16 v[240:241], v7 offset:21504\n\t"
      "buffer_load_dwordx4 v4, s[20:23], s40 offen offset:2048 lds\n\t"
      "v_mfma_f32_16x16x32_bf16 a[4:7], v[216:219], v[100:103], a[4:7]\n\t"
      "ds_read_b64_tr_b16 v[242:243], v7 offset:22016\n\t"
      "v_mfma_f32_16x16x32_bf16 a[36:39], v[164:167], v[100:103], a[36:39]\n\t"
      "ds_read_b64_tr_b16 v[176:177], v8 offset:21504\n\t"
      "v_mfma_f32_16x16x32_bf16 a[36:39], v[216:219], v[104:107], a[36:39]\n\t"
      "ds_read_b64_tr_b16 v[178:179], v8 offset:22016\n\t"
      "v_mfma_f32_16x16x32_bf16 a[68:71], v[164:167], v[104:107], a[68:71]\n\t"
      "ds_read_b64_tr_b16 v[220:221], v9 offset:21504\n\t"
      "buffer_load_dwordx4 v5, s[20:23], s40 offen offset:3072 lds\n\t"
      "v_mfma_f32_16x16x32_bf16 a[68:71], v[216:219], v[108:111], a[68:71]\n\t"
      "ds_read_b64_tr_b16 v[222:223], v9 offset:22016\n\t"
      "v_mfma_f32_16x16x32_bf16 v[48:51], v[164:167], v[108:111], v[48:51]\n\t"
      "v_mfma_f32_16x16x32_bf16 v[48:51], v[216:219], v[112:115], v[48:51]\n\t"
      "s_add_u32 m0, s37, 9216\n\t"
      "s_mov_b32 s40, 13107200\n\t"
      "v_mfma_f32_16x16x32_bf16 a[20:23], v[164:167], v[112:115], a[20:23]\n\t"
      "buffer_load_dwordx4 v2, s[20:23], s40 offen offset:0 lds\n\t"
      "v_mfma_f32_16x16x32_bf16 a[20:23], v[216:219], v[116:119], a[20:23]\n\t"
      "v_mfma_f32_16x16x32_bf16 a[52:55], v[164:167], v[116:119], a[52:55]\n\t"
      "v_mfma_f32_16x16x32_bf16 a[52:55], v[216:219], v[120:123], a[52:55]\n\t"
      "v_mfma_f32_16x16x32_bf16 a[84:87], v[164:167], v[120:123], a[84:87]\n\t"
      "buffer_load_dwordx4 v3, s[20:23], s40 offen offset:1024 lds\n\t"
      "v_mfma_f32_16x16x32_bf16 a[84:87], v[216:219], v[124:127], a[84:87]\n\t"
      "v_mfma_f32_16x16x32_bf16 v[64:67], v[164:167], v[124:127], v[64:67]\n\t"
      "v_mfma_f32_16x16x32_bf16 v[64:67], v[216:219], v[128:131], v[64:67]\n\t"
      "v_mfma_f32_16x16x32_bf16 a[8:11], v[252:255], v[96:99], a[8:11]\n\t"
      "buffer_load_dwordx4 v4, s[20:23], s40 offen offset:2048 lds\n\t"
      "v_mfma_f32_16x16x32_bf16 a[8:11], v[184:187], v[100:103], a[8:11]\n\t"
      "v_mfma_f32_16x16x32_bf16 a[40:43], v[252:255], v[100:103], a[40:43]\n\t"
      "v_mfma_f32_16x16x32_bf16 a[40:43], v[184:187], v[104:107], a[40:43]\n\t"
      "v_mfma_f32_16x16x32_bf16 a[72:75], v[252:255], v[104:107], a[72:75]\n\t"
      "s_waitcnt vmcnt(18)\n\t"
      "ds_read_b64_tr_b16 v[180:181], v6 offset:25600\n\t"
      "buffer_load_dwordx4 v5, s[20:23], s40 offen offset:3072 lds\n\t"
      "v_mfma_f32_16x16x32_bf16 a[72:75], v[184:187], v[108:111], a[72:75]\n\t"
      "ds_read_b64_tr_b16 v[182:183], v6 offset:26112\n\t"
      "v_mfma_f32_16x16x32_bf16 v[52:55], v[252:255], v[108:111], v[52:55]\n\t"
      "ds_read_b64_tr_b16 v[228:229], v7 offset:25600\n\t"
      "v_mfma_f32_16x16x32_bf16 v[52:55], v[184:187], v[112:115], v[52:55]\n\t"
      "ds_read_b64_tr_b16 v[230:231], v7 offset:26112\n\t"
      "s_add_u32 m0, s37, 13312\n\t"
      "s_mov_b32 s40, 14155776\n\t"
      "v_mfma_f32_16x16x32_bf16 a[24:27], v[252:255], v[112:115], a[24:27]\n\t"
      "ds_read_b64_tr_b16 v[244:245], v8 offset:25600\n\t"
      "buffer_load_dwordx4 v2, s[20:23], s40 offen offset:0 lds\n\t"
      "v_mfma_f32_16x16x32_bf16 a[24:27], v[184:187], v[116:119], a[24:27]\n\t"
      "ds_read_b64_tr_b16 v[246:247], v8 offset:26112\n\t"
      "v_mfma_f32_16x16x32_bf16 a[56:59], v[252:255], v[116:119], a[56:59]\n\t"
      "ds_read_b64_tr_b16 v[224:225], v9 offset:25600\n\t"
      "v_mfma_f32_16x16x32_bf16 a[56:59], v[184:187], v[120:123], a[56:59]\n\t"
      "ds_read_b64_tr_b16 v[226:227], v9 offset:26112\n\t"
      "v_mfma_f32_16x16x32_bf16 a[88:91], v[252:255], v[120:123], a[88:91]\n\t"
      "buffer_load_dwordx4 v3, s[20:23], s40 offen offset:1024 lds\n\t"
      "v_mfma_f32_16x16x32_bf16 a[88:91], v[184:187], v[124:127], a[88:91]\n\t"
      "v_mfma_f32_16x16x32_bf16 v[68:71], v[252:255], v[124:127], v[68:71]\n\t"
      "v_mfma_f32_16x16x32_bf16 v[68:71], v[184:187], v[128:131], v[68:71]\n\t"
      "v_mfma_f32_16x16x32_bf16 a[12:15], v[204:207], v[96:99], a[12:15]\n\t"
      "buffer_load_dwordx4 v4, s[20:23], s40 offen offset:2048 lds\n\t"
      "v_mfma_f32_16x16x32_bf16 a[12:15], v[188:191], v[100:103], a[12:15]\n\t"
      "v_mfma_f32_16x16x32_bf16 a[44:47], v[204:207], v[100:103], a[44:47]\n\t"
      "v_mfma_f32_16x16x32_bf16 a[44:47], v[188:191], v[104:107], a[44:47]\n\t"
      "v_mfma_f32_16x16x32_bf16 a[76:79], v[204:207], v[104:107], a[76:79]\n\t"
      "buffer_load_dwordx4 v5, s[20:23], s40 offen offset:3072 lds\n\t"
      "v_mfma_f32_16x16x32_bf16 a[76:79], v[188:191], v[108:111], a[76:79]\n\t"
      "v_mfma_f32_16x16x32_bf16 v[56:59], v[204:207], v[108:111], v[56:59]\n\t"
      "v_mfma_f32_16x16x32_bf16 v[56:59], v[188:191], v[112:115], v[56:59]\n\t"
      "s_add_u32 m0, s37, 17408\n\t"
      "s_mov_b32 s40, 15204352\n\t"
      "v_mfma_f32_16x16x32_bf16 a[28:31], v[204:207], v[112:115], a[28:31]\n\t"
      "s_waitcnt lgkmcnt(0)\n\t"
      "buffer_load_dwordx4 v2, s[20:23], s40 offen offset:0 lds\n\t"
      "v_mfma_f32_16x16x32_bf16 a[28:31], v[188:191], v[116:119], a[28:31]\n\t"
      "v_mfma_f32_16x16x32_bf16 a[60:63], v[204:207], v[116:119], a[60:63]\n\t"
      "s_waitcnt vmcnt(13)\n\t"
      "ds_read_b64_tr_b16 v[232:233], v6 offset:29696\n\t"
      "v_mfma_f32_16x16x32_bf16 a[60:63], v[188:191], v[120:123], a[60:63]\n\t"
      "ds_read_b64_tr_b16 v[234:235], v6 offset:30208\n\t"
      "v_mfma_f32_16x16x32_bf16 a[92:95], v[204:207], v[120:123], a[92:95]\n\t"
      "ds_read_b64_tr_b16 v[168:169], v7 offset:29696\n\t"
      "buffer_load_dwordx4 v3, s[20:23], s40 offen offset:1024 lds\n\t"
      "v_mfma_f32_16x16x32_bf16 a[92:95], v[188:191], v[124:127], a[92:95]\n\t"
      "ds_read_b64_tr_b16 v[170:171], v7 offset:30208\n\t"
      "v_mfma_f32_16x16x32_bf16 v[72:75], v[204:207], v[124:127], v[72:75]\n\t"
      "ds_read_b64_tr_b16 v[172:173], v8 offset:29696\n\t"
      "v_mfma_f32_16x16x32_bf16 v[72:75], v[188:191], v[128:131], v[72:75]\n\t"
      "ds_read_b64_tr_b16 v[174:175], v8 offset:30208\n\t"
      "v_mfma_f32_16x16x32_bf16 a[0:3], v[200:203], v[104:107], a[0:3]\n\t"
      "ds_read_b64_tr_b16 v[208:209], v9 offset:29696\n\t"
      "buffer_load_dwordx4 v4, s[20:23], s40 offen offset:2048 lds\n\t"
      "v_mfma_f32_16x16x32_bf16 a[0:3], v[196:199], v[108:111], a[0:3]\n\t"
      "ds_read_b64_tr_b16 v[210:211], v9 offset:30208\n\t"
      "v_mfma_f32_16x16x32_bf16 a[32:35], v[200:203], v[108:111], a[32:35]\n\t"
      "v_mfma_f32_16x16x32_bf16 a[32:35], v[196:199], v[112:115], a[32:35]\n\t"
      "v_mfma_f32_16x16x32_bf16 a[64:67], v[200:203], v[112:115], a[64:67]\n\t"
      "buffer_load_dwordx4 v5, s[20:23], s40 offen offset:3072 lds\n\t"
      "v_mfma_f32_16x16x32_bf16 a[64:67], v[196:199], v[116:119], a[64:67]\n\t"
      "v_mfma_f32_16x16x32_bf16 v[44:47], v[200:203], v[116:119], v[44:47]\n\t"
      "v_mfma_f32_16x16x32_bf16 v[44:47], v[196:199], v[120:123], v[44:47]\n\t"
      "s_add_u32 m0, s37, 21504\n\t"
      "s_mov_b32 s40, 786432\n\t"
      "v_mfma_f32_16x16x32_bf16 a[16:19], v[200:203], v[120:123], a[16:19]\n\t"
      "buffer_load_dwordx4 v2, s[20:23], s40 offen offset:0 lds\n\t"
      "v_mfma_f32_16x16x32_bf16 a[16:19], v[196:199], v[124:127], a[16:19]\n\t"
      "v_mfma_f32_16x16x32_bf16 a[48:51], v[200:203], v[124:127], a[48:51]\n\t"
      "v_mfma_f32_16x16x32_bf16 a[48:51], v[196:199], v[128:131], a[48:51]\n\t"
      "v_mfma_f32_16x16x32_bf16 a[80:83], v[200:203], v[128:131], a[80:83]\n\t"
      "buffer_load_dwordx4 v3, s[20:23], s40 offen offset:1024 lds\n\t"
      "v_mfma_f32_16x16x32_bf16 a[80:83], v[196:199], v[132:135], a[80:83]\n\t"
      "v_mfma_f32_16x16x32_bf16 v[60:63], v[200:203], v[132:135], v[60:63]\n\t"
      "v_mfma_f32_16x16x32_bf16 v[60:63], v[196:199], v[136:139], v[60:63]\n\t"
      "v_mfma_f32_16x16x32_bf16 a[4:7], v[192:195], v[104:107], a[4:7]\n\t"
      "s_waitcnt vmcnt(14)\n\t"
      "ds_read_b64_tr_b16 v[248:249], v6 offset:33792\n\t"
      "buffer_load_dwordx4 v4, s[20:23], s40 offen offset:2048 lds\n\t"
      "v_mfma_f32_16x16x32_bf16 a[4:7], v[240:243], v[108:111], a[4:7]\n\t"
      "ds_read_b64_tr_b16 v[250:251], v6 offset:34304\n\t"
      "v_mfma_f32_16x16x32_bf16 a[36:39], v[192:195], v[108:111], a[36:39]\n\t"
      "ds_read_b64_tr_b16 v[164:165], v7 offset:33792\n\t"
      "v_mfma_f32_16x16x32_bf16 a[36:39], v[240:243], v[112:115], a[36:39]\n\t"
      "ds_read_b64_tr_b16 v[166:167], v7 offset:34304\n\t"
      "v_mfma_f32_16x16x32_bf16 a[68:71], v[192:195], v[112:115], a[68:71]\n\t"
      "ds_read_b64_tr_b16 v[216:217], v8 offset:33792\n\t"
      "buffer_load_dwordx4 v5, s[20:23], s40 offen offset:3072 lds\n\t"
      "v_mfma_f32_16x16x32_bf16 a[68:71], v[240:243], v[116:119], a[68:71]\n\t"
      "ds_read_b64_tr_b16 v[218:219], v8 offset:34304\n\t"
      "v_mfma_f32_16x16x32_bf16 v[48:51], v[192:195], v[116:119], v[48:51]\n\t"
      "ds_read_b64_tr_b16 v[252:253], v9 offset:33792\n\t"
      "v_mfma_f32_16x16x32_bf16 v[48:51], v[240:243], v[120:123], v[48:51]\n\t"
      "ds_read_b64_tr_b16 v[254:255], v9 offset:34304\n\t"
      "s_add_u32 m0, s37, 25600\n\t"
      "s_mov_b32 s40, 1835008\n\t"
      "v_mfma_f32_16x16x32_bf16 a[20:23], v[192:195], v[120:123], a[20:23]\n\t"
      "buffer_load_dwordx4 v2, s[20:23], s40 offen offset:0 lds\n\t"
      "v_mfma_f32_16x16x32_bf16 a[20:23], v[240:243], v[124:127], a[20:23]\n\t"
      "v_mfma_f32_16x16x32_bf16 a[52:55], v[192:195], v[124:127], a[52:55]\n\t"
      "v_mfma_f32_16x16x32_bf16 a[52:55], v[240:243], v[128:131], a[52:55]\n\t"
      "v_mfma_f32_16x16x32_bf16 a[84:87], v[192:195], v[128:131], a[84:87]\n\t"
      "buffer_load_dwordx4 v3, s[20:23], s40 offen offset:1024 lds\n\t"
      "v_mfma_f32_16x16x32_bf16 a[84:87], v[240:243], v[132:135], a[84:87]\n\t"
      "v_mfma_f32_16x16x32_bf16 v[64:67], v[192:195], v[132:135], v[64:67]\n\t"
      "v_mfma_f32_16x16x32_bf16 v[64:67], v[240:243], v[136:139], v[64:67]\n\t"
      "v_mfma_f32_16x16x32_bf16 a[8:11], v[212:215], v[104:107], a[8:11]\n\t"
      "buffer_load_dwordx4 v4, s[20:23], s40 offen offset:2048 lds\n\t"
      "v_mfma_f32_16x16x32_bf16 a[8:11], v[176:179], v[108:111], a[8:11]\n\t"
      "v_mfma_f32_16x16x32_bf16 a[40:43], v[212:215], v[108:111], a[40:43]\n\t"
      "v_mfma_f32_16x16x32_bf16 a[40:43], v[176:179], v[112:115], a[40:43]\n\t"
      "v_mfma_f32_16x16x32_bf16 a[72:75], v[212:215], v[112:115], a[72:75]\n\t"
      "buffer_load_dwordx4 v5, s[20:23], s40 offen offset:3072 lds\n\t"
      "v_mfma_f32_16x16x32_bf16 a[72:75], v[176:179], v[116:119], a[72:75]\n\t"
      "v_mfma_f32_16x16x32_bf16 v[52:55], v[212:215], v[116:119], v[52:55]\n\t"
      "s_waitcnt vmcnt(16)\n\t"
      "ds_read_b64_tr_b16 v[184:185], v6 offset:9216\n\t"
      "v_mfma_f32_16x16x32_bf16 v[52:55], v[176:179], v[120:123], v[52:55]\n\t"
      "ds_read_b64_tr_b16 v[186:187], v6 offset:9728\n\t"
      "s_add_u32 m0, s37, 29696\n\t"
      "s_mov_b32 s40, 2883584\n\t"
      "v_mfma_f32_16x16x32_bf16 a[24:27], v[212:215], v[120:123], a[24:27]\n\t"
      "ds_read_b64_tr_b16 v[204:205], v7 offset:9216\n\t"
      "s_waitcnt lgkmcnt(3)\n\t"
      "buffer_load_dwordx4 v2, s[20:23], s40 offen offset:0 lds\n\t"
      "v_mfma_f32_16x16x32_bf16 a[24:27], v[176:179], v[124:127], a[24:27]\n\t"
      "ds_read_b64_tr_b16 v[206:207], v7 offset:9728\n\t"
      "v_mfma_f32_16x16x32_bf16 a[56:59], v[212:215], v[124:127], a[56:59]\n\t"
      "ds_read_b64_tr_b16 v[188:189], v8 offset:9216\n\t"
      "v_mfma_f32_16x16x32_bf16 a[56:59], v[176:179], v[128:131], a[56:59]\n\t"
      "ds_read_b64_tr_b16 v[190:191], v8 offset:9728\n\t"
      "v_mfma_f32_16x16x32_bf16 a[88:91], v[212:215], v[128:131], a[88:91]\n\t"
      "ds_read_b64_tr_b16 v[200:201], v9 offset:9216\n\t"
      "buffer_load_dwordx4 v3, s[20:23], s40 offen offset:1024 lds\n\t"
      "v_mfma_f32_16x16x32_bf16 a[88:91], v[176:179], v[132:135], a[88:91]\n\t"
      "ds_read_b64_tr_b16 v[202:203], v9 offset:9728\n\t"
      "v_mfma_f32_16x16x32_bf16 v[68:71], v[212:215], v[132:135], v[68:71]\n\t"
      "v_mfma_f32_16x16x32_bf16 v[68:71], v[176:179], v[136:139], v[68:71]\n\t"
      "v_mfma_f32_16x16x32_bf16 a[12:15], v[236:239], v[104:107], a[12:15]\n\t"
      "buffer_load_dwordx4 v4, s[20:23], s40 offen offset:2048 lds\n\t"
      "v_mfma_f32_16x16x32_bf16 a[12:15], v[220:223], v[108:111], a[12:15]\n\t"
      "v_mfma_f32_16x16x32_bf16 a[44:47], v[236:239], v[108:111], a[44:47]\n\t"
      "v_mfma_f32_16x16x32_bf16 a[44:47], v[220:223], v[112:115], a[44:47]\n\t"
      "v_mfma_f32_16x16x32_bf16 a[76:79], v[236:239], v[112:115], a[76:79]\n\t"
      "buffer_load_dwordx4 v5, s[20:23], s40 offen offset:3072 lds\n\t"
      "v_mfma_f32_16x16x32_bf16 a[76:79], v[220:223], v[116:119], a[76:79]\n\t"
      "v_mfma_f32_16x16x32_bf16 v[56:59], v[236:239], v[116:119], v[56:59]\n\t"
      "v_mfma_f32_16x16x32_bf16 v[56:59], v[220:223], v[120:123], v[56:59]\n\t"
      "s_add_u32 m0, s37, 33792\n\t"
      "s_mov_b32 s40, 3932160\n\t"
      "v_mfma_f32_16x16x32_bf16 a[28:31], v[236:239], v[120:123], a[28:31]\n\t"
      "buffer_load_dwordx4 v2, s[20:23], s40 offen offset:0 lds\n\t"
      "v_mfma_f32_16x16x32_bf16 a[28:31], v[220:223], v[124:127], a[28:31]\n\t"
      "v_mfma_f32_16x16x32_bf16 a[60:63], v[236:239], v[124:127], a[60:63]\n\t"
      "v_mfma_f32_16x16x32_bf16 a[60:63], v[220:223], v[128:131], a[60:63]\n\t"
      "v_mfma_f32_16x16x32_bf16 a[92:95], v[236:239], v[128:131], a[92:95]\n\t"
      "s_waitcnt vmcnt(17)\n\t"
      "ds_read_b64_tr_b16 v[196:197], v6 offset:13312\n\t"
      "buffer_load_dwordx4 v3, s[20:23], s40 offen offset:1024 lds\n\t"
      "v_mfma_f32_16x16x32_bf16 a[92:95], v[220:223], v[132:135], a[92:95]\n\t"
      "ds_read_b64_tr_b16 v[198:199], v6 offset:13824\n\t"
      "v_mfma_f32_16x16x32_bf16 v[72:75], v[236:239], v[132:135], v[72:75]\n\t"
      "ds_read_b64_tr_b16 v[192:193], v7 offset:13312\n\t"
      "v_mfma_f32_16x16x32_bf16 v[72:75], v[220:223], v[136:139], v[72:75]\n\t"
      "ds_read_b64_tr_b16 v[194:195], v7 offset:13824\n\t"
      "v_mfma_f32_16x16x32_bf16 a[0:3], v[180:183], v[112:115], a[0:3]\n\t"
      "ds_read_b64_tr_b16 v[240:241], v8 offset:13312\n\t"
      "buffer_load_dwordx4 v4, s[20:23], s40 offen offset:2048 lds\n\t"
      "v_mfma_f32_16x16x32_bf16 a[0:3], v[232:235], v[116:119], a[0:3]\n\t"
      "ds_read_b64_tr_b16 v[242:243], v8 offset:13824\n\t"
      "s_mov_b32 m0, s101\n\t"
      "v_mfma_f32_16x16x32_bf16 a[32:35], v[180:183], v[116:119], a[32:35]\n\t"
      "ds_read_b64_tr_b16 v[212:213], v9 offset:13312\n\t"
      "v_mfma_f32_16x16x32_bf16 a[32:35], v[232:235], v[120:123], a[32:35]\n\t"
      "ds_read_b64_tr_b16 v[214:215], v9 offset:13824\n\t"
      "v_mfma_f32_16x16x32_bf16 a[64:67], v[180:183], v[120:123], a[64:67]\n\t"
      "buffer_load_dwordx4 v27, s[16:19], s54 offen offset:0 lds\n\t"
      "v_mfma_f32_16x16x32_bf16 a[64:67], v[232:235], v[124:127], a[64:67]\n\t"
      "v_mfma_f32_16x16x32_bf16 v[44:47], v[180:183], v[124:127], v[44:47]\n\t"
      "v_mfma_f32_16x16x32_bf16 v[44:47], v[232:235], v[128:131], v[44:47]\n\t"
      "s_mov_b32 m0, s92\n\t"
      "v_mfma_f32_16x16x32_bf16 a[16:19], v[180:183], v[128:131], a[16:19]\n\t"
      "buffer_load_dwordx4 v28, s[16:19], s54 offen offset:0 lds\n\t"
      "v_mfma_f32_16x16x32_bf16 a[16:19], v[232:235], v[132:135], a[16:19]\n\t"
      "v_mfma_f32_16x16x32_bf16 a[48:51], v[180:183], v[132:135], a[48:51]\n\t"
      "v_mfma_f32_16x16x32_bf16 a[48:51], v[232:235], v[136:139], a[48:51]\n\t"
      "v_mfma_f32_16x16x32_bf16 a[80:83], v[180:183], v[136:139], a[80:83]\n\t"
      "buffer_load_dwordx4 v29, s[16:19], s54 offen offset:1024 lds\n\t"
      "v_mfma_f32_16x16x32_bf16 a[80:83], v[232:235], v[140:143], a[80:83]\n\t"
      "v_mfma_f32_16x16x32_bf16 v[60:63], v[180:183], v[140:143], v[60:63]\n\t"
      "v_mfma_f32_16x16x32_bf16 v[60:63], v[232:235], v[144:147], v[60:63]\n\t"
      "v_mfma_f32_16x16x32_bf16 a[4:7], v[228:231], v[112:115], a[4:7]\n\t"
      "buffer_load_dwordx4 v30, s[16:19], s54 offen offset:2048 lds\n\t"
      "v_mfma_f32_16x16x32_bf16 a[4:7], v[168:171], v[116:119], a[4:7]\n\t"
      "v_mfma_f32_16x16x32_bf16 a[36:39], v[228:231], v[116:119], a[36:39]\n\t"
      "s_waitcnt vmcnt(19)\n\t"
      "ds_read_b64_tr_b16 v[176:177], v6 offset:17408\n\t"
      "v_mfma_f32_16x16x32_bf16 a[36:39], v[168:171], v[120:123], a[36:39]\n\t"
      "ds_read_b64_tr_b16 v[178:179], v6 offset:17920\n\t"
      "v_mfma_f32_16x16x32_bf16 a[68:71], v[228:231], v[120:123], a[68:71]\n\t"
      "ds_read_b64_tr_b16 v[236:237], v7 offset:17408\n\t"
      "s_add_u32 m0, s37, 33792\n\t"
      "s_nop 0\n\t"
      "buffer_load_dwordx4 v5, s[20:23], s40 offen offset:3072 lds\n\t"
      "v_mfma_f32_16x16x32_bf16 a[68:71], v[168:171], v[124:127], a[68:71]\n\t"
      "ds_read_b64_tr_b16 v[238:239], v7 offset:17920\n\t"
      "v_mfma_f32_16x16x32_bf16 v[48:51], v[228:231], v[124:127], v[48:51]\n\t"
      "ds_read_b64_tr_b16 v[220:221], v8 offset:17408\n\t"
      "v_mfma_f32_16x16x32_bf16 v[48:51], v[168:171], v[128:131], v[48:51]\n\t"
      "ds_read_b64_tr_b16 v[222:223], v8 offset:17920\n\t"
      "s_add_u32 m0, s37, 9216\n\t"
      "s_mov_b32 s40, 4980736\n\t"
      "v_mfma_f32_16x16x32_bf16 a[20:23], v[228:231], v[128:131], a[20:23]\n\t"
      "ds_read_b64_tr_b16 v[180:181], v9 offset:17408\n\t"
      "s_waitcnt lgkmcnt(5)\n\t"
      "buffer_load_dwordx4 v2, s[20:23], s40 offen offset:0 lds\n\t"
      "v_mfma_f32_16x16x32_bf16 a[20:23], v[168:171], v[132:135], a[20:23]\n\t"
      "ds_read_b64_tr_b16 v[182:183], v9 offset:17920\n\t"
      "v_mfma_f32_16x16x32_bf16 a[52:55], v[228:231], v[132:135], a[52:55]\n\t"
      "s_add_u32 s41, s36, 1\n\t"
      "v_mfma_f32_16x16x32_bf16 a[52:55], v[168:171], v[136:139], a[52:55]\n\t"
      "s_and_b32 s42, s41, 3\n\t"
      "v_mfma_f32_16x16x32_bf16 a[84:87], v[228:231], v[136:139], a[84:87]\n\t"
      "buffer_load_dwordx4 v3, s[20:23], s40 offen offset:1024 lds\n\t"
      "v_mfma_f32_16x16x32_bf16 a[84:87], v[168:171], v[140:143], a[84:87]\n\t"
      "s_mul_i32 s42, s42, 40960\n\t"
      "v_mfma_f32_16x16x32_bf16 v[64:67], v[228:231], v[140:143], v[64:67]\n\t"
      "s_add_u32 s42, s42, %3\n\t"
      "v_mfma_f32_16x16x32_bf16 v[64:67], v[168:171], v[144:147], v[64:67]\n\t"
      "s_cmp_ge_u32 s41, 4\n\t"
      "s_cselect_b32 s41, 0, 2048\n\t"
      "s_cselect_b32 s43, 2048, 16\n\t"
      "v_mfma_f32_16x16x32_bf16 a[8:11], v[244:247], v[112:115], a[8:11]\n\t"
      "buffer_load_dwordx4 v4, s[20:23], s40 offen offset:2048 lds\n\t"
      "v_mfma_f32_16x16x32_bf16 a[8:11], v[172:175], v[116:119], a[8:11]\n\t"
      "s_add_u32 s41, s41, s42\n\t"
      "v_mfma_f32_16x16x32_bf16 a[40:43], v[244:247], v[116:119], a[40:43]\n\t"
      "s_add_u32 s43, s43, s42\n\t"
      "v_mfma_f32_16x16x32_bf16 a[40:43], v[172:175], v[120:123], a[40:43]\n\t"
      "v_add_u32 v22, s41, v31\n\t"
      "v_mfma_f32_16x16x32_bf16 a[72:75], v[244:247], v[120:123], a[72:75]\n\t"
      "buffer_load_dwordx4 v5, s[20:23], s40 offen offset:3072 lds\n\t"
      "v_mfma_f32_16x16x32_bf16 a[72:75], v[172:175], v[124:127], a[72:75]\n\t"
      "v_add_u32 v23, s43, v31\n\t"
      "v_mfma_f32_16x16x32_bf16 v[52:55], v[244:247], v[124:127], v[52:55]\n\t"
      "s_add_u32 s41, s36, 2\n\t"
      "v_mfma_f32_16x16x32_bf16 v[52:55], v[172:175], v[128:131], v[52:55]\n\t"
      "s_add_u32 m0, s37, 13312\n\t"
      "s_mov_b32 s40, 6029312\n\t"
      "v_mfma_f32_16x16x32_bf16 a[24:27], v[244:247], v[128:131], a[24:27]\n\t"
      "s_waitcnt vmcnt(20)\n\t"
      "ds_read_b64_tr_b16 v[232:233], v6 offset:21504\n\t"
      "buffer_load_dwordx4 v2, s[20:23], s40 offen offset:0 lds\n\t"
      "v_mfma_f32_16x16x32_bf16 a[24:27], v[172:175], v[132:135], a[24:27]\n\t"
      "ds_read_b64_tr_b16 v[234:235], v6 offset:22016\n\t"
      "v_mfma_f32_16x16x32_bf16 a[56:59], v[244:247], v[132:135], a[56:59]\n\t"
      "ds_read_b64_tr_b16 v[228:229], v7 offset:21504\n\t"
      "v_mfma_f32_16x16x32_bf16 a[56:59], v[172:175], v[136:139], a[56:59]\n\t"
      "ds_read_b64_tr_b16 v[230:231], v7 offset:22016\n\t"
      "v_mfma_f32_16x16x32_bf16 a[88:91], v[244:247], v[136:139], a[88:91]\n\t"
      "ds_read_b64_tr_b16 a[184:185], v8 offset:21504\n\t"
      "buffer_load_dwordx4 v3, s[20:23], s40 offen offset:1024 lds\n\t"
      "v_mfma_f32_16x16x32_bf16 a[88:91], v[172:175], v[140:143], a[88:91]\n\t"
      "ds_read_b64_tr_b16 a[186:187], v8 offset:22016\n\t"
      "v_mfma_f32_16x16x32_bf16 v[68:71], v[244:247], v[140:143], v[68:71]\n\t"
      "ds_read_b64_tr_b16 a[188:189], v9 offset:21504\n\t"
      "v_mfma_f32_16x16x32_bf16 v[68:71], v[172:175], v[144:147], v[68:71]\n\t"
      "ds_read_b64_tr_b16 a[190:191], v9 offset:22016\n\t"
      "v_mfma_f32_16x16x32_bf16 a[12:15], v[224:227], v[112:115], a[12:15]\n\t"
      "buffer_load_dwordx4 v4, s[20:23], s40 offen offset:2048 lds\n\t"
      "v_mfma_f32_16x16x32_bf16 a[12:15], v[208:211], v[116:119], a[12:15]\n\t"
      "s_and_b32 s42, s41, 3\n\t"
      "v_mfma_f32_16x16x32_bf16 a[44:47], v[224:227], v[116:119], a[44:47]\n\t"
      "s_mul_i32 s42, s42, 40960\n\t"
      "v_mfma_f32_16x16x32_bf16 a[44:47], v[208:211], v[120:123], a[44:47]\n\t"
      "s_add_u32 s42, s42, %3\n\t"
      "v_mfma_f32_16x16x32_bf16 a[76:79], v[224:227], v[120:123], a[76:79]\n\t"
      "buffer_load_dwordx4 v5, s[20:23], s40 offen offset:3072 lds\n\t"
      "v_mfma_f32_16x16x32_bf16 a[76:79], v[208:211], v[124:127], a[76:79]\n\t"
      "s_cmp_ge_u32 s41, 4\n\t"
      "s_cselect_b32 s41, 0, 2048\n\t"
      "s_cselect_b32 s43, 2048, 16\n\t"
      "v_mfma_f32_16x16x32_bf16 v[56:59], v[224:227], v[124:127], v[56:59]\n\t"
      "s_add_u32 s41, s41, s42\n\t"
      "v_mfma_f32_16x16x32_bf16 v[56:59], v[208:211], v[128:131], v[56:59]\n\t"
      "s_add_u32 m0, s37, 17408\n\t"
      "s_mov_b32 s40, 7077888\n\t"
      "v_mfma_f32_16x16x32_bf16 a[28:31], v[224:227], v[128:131], a[28:31]\n\t"
      "s_waitcnt lgkmcnt(0)\n\t"
      "buffer_load_dwordx4 v2, s[20:23], s40 offen offset:0 lds\n\t"
      "v_mfma_f32_16x16x32_bf16 a[28:31], v[208:211], v[132:135], a[28:31]\n\t"
      "s_add_u32 s43, s43, s42\n\t"
      "v_mfma_f32_16x16x32_bf16 a[60:63], v[224:227], v[132:135], a[60:63]\n\t"
      "v_add_u32 v24, s41, v31\n\t"
      "v_mfma_f32_16x16x32_bf16 a[60:63], v[208:211], v[136:139], a[60:63]\n\t"
      "v_add_u32 v25, s43, v31\n\t"
      "v_mfma_f32_16x16x32_bf16 a[92:95], v[224:227], v[136:139], a[92:95]\n\t"
      "buffer_load_dwordx4 v3, s[20:23], s40 offen offset:1024 lds\n\t"
      "v_mfma_f32_16x16x32_bf16 a[92:95], v[208:211], v[140:143], a[92:95]\n\t"
      "s_add_u32 s41, s36, 3\n\t"
      "v_mfma_f32_16x16x32_bf16 v[72:75], v[224:227], v[140:143], v[72:75]\n\t"
      "s_waitcnt vmcnt(22)\n\t"
      "ds_read_b64_tr_b16 v[168:169], v6 offset:25600\n\t"
      "v_mfma_f32_16x16x32_bf16 v[72:75], v[208:211], v[144:147], v[72:75]\n\t"
      "ds_read_b64_tr_b16 v[170:171], v6 offset:26112\n\t"
      "v_mfma_f32_16x16x32_bf16 a[0:3], v[248:251], v[120:123], a[0:3]\n\t"
      "ds_read_b64_tr_b16 v[244:245], v7 offset:25600\n\t"
      "buffer_load_dwordx4 v4, s[20:23], s40 offen offset:2048 lds\n\t"
      "v_mfma_f32_16x16x32_bf16 a[0:3], v[184:187], v[124:127], a[0:3]\n\t"
      "ds_read_b64_tr_b16 v[246:247], v7 offset:26112\n\t"
      "v_mfma_f32_16x16x32_bf16 a[32:35], v[248:251], v[124:127], a[32:35]\n\t"
      "ds_read_b64_tr_b16 a[192:193], v8 offset:25600\n\t"
      "v_mfma_f32_16x16x32_bf16 a[32:35], v[184:187], v[128:131], a[32:35]\n\t"
      "ds_read_b64_tr_b16 a[194:195], v8 offset:26112\n\t"
      "v_mfma_f32_16x16x32_bf16 a[64:67], v[248:251], v[128:131], a[64:67]\n\t"
      "ds_read_b64_tr_b16 a[196:197], v9 offset:25600\n\t"
      "buffer_load_dwordx4 v5, s[20:23], s40 offen offset:3072 lds\n\t"
      "v_mfma_f32_16x16x32_bf16 a[64:67], v[184:187], v[132:135], a[64:67]\n\t"
      "ds_read_b64_tr_b16 a[198:199], v9 offset:26112\n\t"
      "v_mfma_f32_16x16x32_bf16 v[44:47], v[248:251], v[132:135], v[44:47]\n\t"
      "s_waitcnt vmcnt(13)\n\t"
      "ds_read_b128 a[96:99], v10 offset:0\n\t"
      "v_mfma_f32_16x16x32_bf16 v[44:47], v[184:187], v[136:139], v[44:47]\n\t"
      "s_add_u32 m0, s37, 21504\n\t"
      "s_mov_b32 s40, 8126464\n\t"
      "v_mfma_f32_16x16x32_bf16 a[16:19], v[248:251], v[136:139], a[16:19]\n\t"
      "buffer_load_dwordx4 v2, s[20:23], s40 offen offset:0 lds\n\t"
      "v_mfma_f32_16x16x32_bf16 a[16:19], v[184:187], v[140:143], a[16:19]\n\t"
      "s_and_b32 s42, s41, 3\n\t"
      "v_mfma_f32_16x16x32_bf16 a[48:51], v[248:251], v[140:143], a[48:51]\n\t"
      "ds_read_b128 a[100:103], v11 offset:0\n\t"
      "v_mfma_f32_16x16x32_bf16 a[48:51], v[184:187], v[144:147], a[48:51]\n\t"
      "s_mul_i32 s42, s42, 40960\n\t"
      "v_mfma_f32_16x16x32_bf16 a[80:83], v[248:251], v[144:147], a[80:83]\n\t"
      "buffer_load_dwordx4 v3, s[20:23], s40 offen offset:1024 lds\n\t"
      "v_mfma_f32_16x16x32_bf16 a[80:83], v[184:187], v[148:151], a[80:83]\n\t"
      "s_add_u32 s42, s42, %3\n\t"
      "v_mfma_f32_16x16x32_bf16 v[60:63], v[248:251], v[148:151], v[60:63]\n\t"
      "ds_read_b128 a[104:107], v12 offset:0\n\t"
      "v_mfma_f32_16x16x32_bf16 v[60:63], v[184:187], v[152:155], v[60:63]\n\t"
      "s_cmp_ge_u32 s41, 4\n\t"
      "s_cselect_b32 s41, 0, 2048\n\t"
      "s_cselect_b32 s43, 2048, 16\n\t"
      "v_mfma_f32_16x16x32_bf16 a[4:7], v[164:167], v[120:123], a[4:7]\n\t"
      "buffer_load_dwordx4 v4, s[20:23], s40 offen offset:2048 lds\n\t"
      "v_mfma_f32_16x16x32_bf16 a[4:7], v[204:207], v[124:127], a[4:7]\n\t"
      "s_add_u32 s41, s41, s42\n\t"
      "v_mfma_f32_16x16x32_bf16 a[36:39], v[164:167], v[124:127], a[36:39]\n\t"
      "ds_read_b128 a[108:111], v13 offset:0\n\t"
      "v_mfma_f32_16x16x32_bf16 a[36:39], v[204:207], v[128:131], a[36:39]\n\t"
      "s_add_u32 s43, s43, s42\n\t"
      "v_mfma_f32_16x16x32_bf16 a[68:71], v[164:167], v[128:131], a[68:71]\n\t"
      "ds_read_b64_tr_b16 v[172:173], v6 offset:29696\n\t"
      "buffer_load_dwordx4 v5, s[20:23], s40 offen offset:3072 lds\n\t"
      "v_mfma_f32_16x16x32_bf16 a[68:71], v[204:207], v[132:135], a[68:71]\n\t"
      "ds_read_b64_tr_b16 v[174:175], v6 offset:30208\n\t"
      "v_mfma_f32_16x16x32_bf16 v[48:51], v[164:167], v[132:135], v[48:51]\n\t"
      "ds_read_b64_tr_b16 v[224:225], v7 offset:29696\n\t"
      "v_mfma_f32_16x16x32_bf16 v[48:51], v[204:207], v[136:139], v[48:51]\n\t"
      "ds_read_b64_tr_b16 v[226:227], v7 offset:30208\n\t"
      "s_add_u32 m0, s37, 25600\n\t"
      "s_mov_b32 s40, 9175040\n\t"
      "v_mfma_f32_16x16x32_bf16 a[20:23], v[164:167], v[136:139], a[20:23]\n\t"
      "ds_read_b64_tr_b16 a[200:201], v8 offset:29696\n\t"
      "s_waitcnt lgkmcnt(5)\n\t"
      "buffer_load_dwordx4 v2, s[20:23], s40 offen offset:0 lds\n\t"
      "v_mfma_f32_16x16x32_bf16 a[20:23], v[204:207], v[140:143], a[20:23]\n\t"
      "ds_read_b64_tr_b16 a[202:203], v8 offset:30208\n\t"
      "v_mfma_f32_16x16x32_bf16 a[52:55], v[164:167], v[140:143], a[52:55]\n\t"
      "ds_read_b64_tr_b16 a[204:205], v9 offset:29696\n\t"
      "v_mfma_f32_16x16x32_bf16 a[52:55], v[204:207], v[144:147], a[52:55]\n\t"
      "ds_read_b64_tr_b16 a[206:207], v9 offset:30208\n\t"
      "v_mfma_f32_16x16x32_bf16 a[84:87], v[164:167], v[144:147], a[84:87]\n\t"
      "ds_read_b128 a[112:115], v10 offset:256\n\t"
      "buffer_load_dwordx4 v3, s[20:23], s40 offen offset:1024 lds\n\t"
      "v_mfma_f32_16x16x32_bf16 a[84:87], v[204:207], v[148:151], a[84:87]\n\t"
      "v_add_u32 v26, s41, v31\n\t"
      "v_mfma_f32_16x16x32_bf16 v[64:67], v[164:167], v[148:151], v[64:67]\n\t"
      "v_add_u32 v27, s43, v31\n\t"
      "v_mfma_f32_16x16x32_bf16 v[64:67], v[204:207], v[152:155], v[64:67]\n\t"
      "s_lshl_b32 s41, s36, 1\n\t"
      "v_mfma_f32_16x16x32_bf16 a[8:11], v[216:219], v[120:123], a[8:11]\n\t"
      "ds_read_b128 a[116:119], v11 offset:256\n\t"
      "buffer_load_dwordx4 v4, s[20:23], s40 offen offset:2048 lds\n\t"
      "v_mfma_f32_16x16x32_bf16 a[8:11], v[188:191], v[124:127], a[8:11]\n\t"
      "s_sub_u32 s41, s58, s41\n\t"
      "v_mfma_f32_16x16x32_bf16 a[40:43], v[216:219], v[124:127], a[40:43]\n\t"
      "s_add_u32 s41, s41, 6\n\t"
      "v_mfma_f32_16x16x32_bf16 a[40:43], v[188:191], v[128:131], a[40:43]\n\t"
      "s_lshl_b32 s41, s41, 10\n\t"
      "v_mfma_f32_16x16x32_bf16 a[72:75], v[216:219], v[128:131], a[72:75]\n\t"
      "ds_read_b128 a[120:123], v12 offset:256\n\t"
      "buffer_load_dwordx4 v5, s[20:23], s40 offen offset:3072 lds\n\t"
      "v_mfma_f32_16x16x32_bf16 a[72:75], v[188:191], v[132:135], a[72:75]\n\t"
      "s_lshl_b32 s42, s38, 7\n\t"
      "v_mfma_f32_16x16x32_bf16 v[52:55], v[216:219], v[132:135], v[52:55]\n\t"
      "s_add_u32 s41, s41, s42\n\t"
      "v_mfma_f32_16x16x32_bf16 v[52:55], v[188:191], v[136:139], v[52:55]\n\t"
      "s_add_u32 m0, s37, 29696\n\t"
      "s_mov_b32 s40, 10223616\n\t"
      "v_mfma_f32_16x16x32_bf16 a[24:27], v[216:219], v[136:139], a[24:27]\n\t"
      "ds_read_b128 a[124:127], v13 offset:256\n\t"
      "v_mfma_f32_16x16x32_bf16 a[24:27], v[188:191], v[140:143], a[24:27]\n\t"
      "s_waitcnt lgkmcnt(1)\n\t"
      "buffer_load_dwordx4 v2, s[20:23], s40 offen offset:0 lds\n\t"
      "v_mfma_f32_16x16x32_bf16 a[56:59], v[216:219], v[140:143], a[56:59]\n\t"
      "s_waitcnt vmcnt(21)\n\t"
      "ds_read_b64_tr_b16 v[208:209], v6 offset:33792\n\t"
      "v_mfma_f32_16x16x32_bf16 a[56:59], v[188:191], v[144:147], a[56:59]\n\t"
      "ds_read_b64_tr_b16 v[210:211], v6 offset:34304\n\t"
      "v_mfma_f32_16x16x32_bf16 a[88:91], v[216:219], v[144:147], a[88:91]\n\t"
      "ds_read_b64_tr_b16 v[248:249], v7 offset:33792\n\t"
      "v_mfma_f32_16x16x32_bf16 a[88:91], v[188:191], v[148:151], a[88:91]\n\t"
      "ds_read_b64_tr_b16 v[250:251], v7 offset:34304\n\t"
      "v_mfma_f32_16x16x32_bf16 v[68:71], v[216:219], v[148:151], v[68:71]\n\t"
      "ds_read_b64_tr_b16 a[208:209], v8 offset:33792\n\t"
      "v_mfma_f32_16x16x32_bf16 v[68:71], v[188:191], v[152:155], v[68:71]\n\t"
      "ds_read_b64_tr_b16 a[210:211], v8 offset:34304\n\t"
      "v_mfma_f32_16x16x32_bf16 a[12:15], v[252:255], v[120:123], a[12:15]\n\t"
      "ds_read_b64_tr_b16 a[212:213], v9 offset:33792\n\t"
      "v_mfma_f32_16x16x32_bf16 a[12:15], v[200:203], v[124:127], a[12:15]\n\t"
      "ds_read_b64_tr_b16 a[214:215], v9 offset:34304\n\t"
      "v_mfma_f32_16x16x32_bf16 a[44:47], v[252:255], v[124:127], a[44:47]\n\t"
      "ds_read_b128 a[128:131], v14 offset:0\n\t"
      "v_mfma_f32_16x16x32_bf16 a[44:47], v[200:203], v[128:131], a[44:47]\n\t"
      "buffer_load_dwordx4 v3, s[20:23], s40 offen offset:1024 lds\n\t"
      "v_mfma_f32_16x16x32_bf16 a[76:79], v[252:255], v[128:131], a[76:79]\n\t"
      "s_xor_b32 s42, s39, 1\n\t"
      "v_mfma_f32_16x16x32_bf16 a[76:79], v[200:203], v[132:135], a[76:79]\n\t"
      "s_lshl_b32 s42, s42, 6\n\t"
      "v_mfma_f32_16x16x32_bf16 v[56:59], v[252:255], v[132:135], v[56:59]\n\t"
      "ds_read_b128 a[132:135], v15 offset:0\n\t"
      "v_mfma_f32_16x16x32_bf16 v[56:59], v[200:203], v[136:139], v[56:59]\n\t"
      "buffer_load_dwordx4 v4, s[20:23], s40 offen offset:2048 lds\n\t"
      "v_mfma_f32_16x16x32_bf16 a[28:31], v[252:255], v[136:139], a[28:31]\n\t"
      "s_add_u32 s41, s41, s42\n\t"
      "v_mfma_f32_16x16x32_bf16 a[28:31], v[200:203], v[140:143], a[28:31]\n\t"
      "v_and_b32 v42, 15, v1\n\t"
      "v_mfma_f32_16x16x32_bf16 a[60:63], v[252:255], v[140:143], a[60:63]\n\t"
      "ds_read_b128 a[136:139], v16 offset:0\n\t"
      "v_mfma_f32_16x16x32_bf16 a[60:63], v[200:203], v[144:147], a[60:63]\n\t"
      "buffer_load_dwordx4 v5, s[20:23], s40 offen offset:3072 lds\n\t"
      "v_mfma_f32_16x16x32_bf16 a[92:95], v[252:255], v[144:147], a[92:95]\n\t"
      "v_lshrrev_b32 v43, 4, v1\n\t"
      "v_mfma_f32_16x16x32_bf16 a[92:95], v[200:203], v[148:151], a[92:95]\n\t"
      "v_lshlrev_b32 v43, 3, v43\n\t"
      "v_mfma_f32_16x16x32_bf16 v[72:75], v[252:255], v[148:151], v[72:75]\n\t"
      "ds_read_b128 a[140:143], v17 offset:0\n\t"
      "v_mfma_f32_16x16x32_bf16 v[72:75], v[200:203], v[152:155], v[72:75]\n\t"
      "s_add_u32 m0, s37, 33792\n\t"
      "s_mov_b32 s40, 11272192\n\t"
      "v_mfma_f32_16x16x32_bf16 a[0:3], v[196:199], v[128:131], a[0:3]\n\t"
      "s_waitcnt vmcnt(20)\n\t"
      "ds_read_b64_tr_b16 v[184:185], v6 offset:9216\n\t"
      "v_mfma_f32_16x16x32_bf16 a[0:3], v[176:179], v[132:135], a[0:3]\n\t"
      "ds_read_b64_tr_b16 v[186:187], v6 offset:9728\n\t"
      "v_mfma_f32_16x16x32_bf16 a[32:35], v[196:199], v[132:135], a[32:35]\n\t"
      "ds_read_b64_tr_b16 v[164:165], v7 offset:9216\n\t"
      "v_mfma_f32_16x16x32_bf16 a[32:35], v[176:179], v[136:139], a[32:35]\n\t"
      "ds_read_b64_tr_b16 v[166:167], v7 offset:9728\n\t"
      "v_mfma_f32_16x16x32_bf16 a[64:67], v[196:199], v[136:139], a[64:67]\n\t"
      "ds_read_b64_tr_b16 a[216:217], v8 offset:9216\n\t"
      "v_mfma_f32_16x16x32_bf16 a[64:67], v[176:179], v[140:143], a[64:67]\n\t"
      "ds_read_b64_tr_b16 a[218:219], v8 offset:9728\n\t"
      "v_mfma_f32_16x16x32_bf16 v[44:47], v[196:199], v[140:143], v[44:47]\n\t"
      "ds_read_b64_tr_b16 a[220:221], v9 offset:9216\n\t"
      "v_mfma_f32_16x16x32_bf16 v[44:47], v[176:179], v[144:147], v[44:47]\n\t"
      "ds_read_b64_tr_b16 a[222:223], v9 offset:9728\n\t"
      "v_mfma_f32_16x16x32_bf16 a[16:19], v[196:199], v[144:147], a[16:19]\n\t"
      "ds_read_b128 a[144:147], v14 offset:256\n\t"
      "v_mfma_f32_16x16x32_bf16 a[16:19], v[176:179], v[148:151], a[16:19]\n\t"
      "s_waitcnt lgkmcnt(4)\n\t"
      "buffer_load_dwordx4 v2, s[20:23], s40 offen offset:0 lds\n\t"
      "v_mfma_f32_16x16x32_bf16 a[48:51], v[196:199], v[148:151], a[48:51]\n\t"
      "v_lshl_add_u32 v43, v42, 14, v43\n\t"
      "v_mfma_f32_16x16x32_bf16 a[48:51], v[176:179], v[152:155], a[48:51]\n\t"
      "v_add_u32 v28, s41, v43\n\t"
      "v_mfma_f32_16x16x32_bf16 a[80:83], v[196:199], v[152:155], a[80:83]\n\t"
      "ds_read_b128 a[148:151], v15 offset:256\n\t"
      "v_mfma_f32_16x16x32_bf16 a[80:83], v[176:179], v[156:159], a[80:83]\n\t"
      "buffer_load_dwordx4 v3, s[20:23], s40 offen offset:1024 lds\n\t"
      "v_mfma_f32_16x16x32_bf16 v[60:63], v[196:199], v[156:159], v[60:63]\n\t"
      "s_cmp_eq_u32 s36, 3\n\t"
      "s_cselect_b32 s42, 16, 15\n\t"
      "v_mfma_f32_16x16x32_bf16 v[60:63], v[176:179], v[160:163], v[60:63]\n\t"
      "v_cmp_gt_u32 vcc, s42, v42\n\t"
      "v_add_u32 v29, 0x2000, v28\n\t"
      "v_bfrev_b32 v43, 1\n\t"
      "v_cndmask_b32 v29, v43, v29, vcc\n\t"
      "s_sub_u32 s8, s49, 251658240\n\t"
      "s_cmp_eq_u32 s39, 0\n\t"
      "s_cselect_b32 s9, 0xb8000000, 0\n\t"
      "v_mov_b32 v40, s9\n\t"
      "v_mfma_f32_16x16x32_bf16 a[4:7], v[192:195], v[128:131], a[4:7]\n\t"
      "ds_read_b128 a[152:155], v16 offset:256\n\t"
      "v_mfma_f32_16x16x32_bf16 a[4:7], v[236:239], v[132:135], a[4:7]\n\t"
      "buffer_load_dwordx4 v4, s[20:23], s40 offen offset:2048 lds\n\t"
      "v_mfma_f32_16x16x32_bf16 a[36:39], v[192:195], v[132:135], a[36:39]\n\t"
      "v_mfma_f32_16x16x32_bf16 a[36:39], v[236:239], v[136:139], a[36:39]\n\t"
      "v_mfma_f32_16x16x32_bf16 a[68:71], v[192:195], v[136:139], a[68:71]\n\t"
      "ds_read_b128 a[156:159], v17 offset:256\n\t"
      "v_mfma_f32_16x16x32_bf16 a[68:71], v[236:239], v[140:143], a[68:71]\n\t"
      "buffer_load_dwordx4 v5, s[20:23], s40 offen offset:3072 lds\n\t"
      "v_mfma_f32_16x16x32_bf16 v[48:51], v[192:195], v[140:143], v[48:51]\n\t"
      "s_waitcnt vmcnt(20)\n\t"
      "ds_read_b64_tr_b16 v[204:205], v6 offset:13312\n\t"
      "v_mfma_f32_16x16x32_bf16 v[48:51], v[236:239], v[144:147], v[48:51]\n\t"
      "ds_read_b64_tr_b16 v[206:207], v6 offset:13824\n\t"
      "v_mfma_f32_16x16x32_bf16 a[20:23], v[192:195], v[144:147], a[20:23]\n\t"
      "ds_read_b64_tr_b16 v[216:217], v7 offset:13312\n\t"
      "s_add_u32 m0, s37, 9216\n\t"
      "s_mov_b32 s40, 12320768\n\t"
      "v_mfma_f32_16x16x32_bf16 a[20:23], v[236:239], v[148:151], a[20:23]\n\t"
      "ds_read_b64_tr_b16 v[218:219], v7 offset:13824\n\t"
      "s_waitcnt lgkmcnt(4)\n\t"
      "buffer_load_dwordx4 v2, s[20:23], s40 offen offset:0 lds\n\t"
      "v_mfma_f32_16x16x32_bf16 a[52:55], v[192:195], v[148:151], a[52:55]\n\t"
      "ds_read_b64_tr_b16 a[224:225], v8 offset:13312\n\t"
      "v_mfma_f32_16x16x32_bf16 a[52:55], v[236:239], v[152:155], a[52:55]\n\t"
      "ds_read_b64_tr_b16 a[226:227], v8 offset:13824\n\t"
      "v_mfma_f32_16x16x32_bf16 a[84:87], v[192:195], v[152:155], a[84:87]\n\t"
      "ds_read_b64_tr_b16 a[228:229], v9 offset:13312\n\t"
      "v_mfma_f32_16x16x32_bf16 a[84:87], v[236:239], v[156:159], a[84:87]\n\t"
      "ds_read_b64_tr_b16 a[230:231], v9 offset:13824\n\t"
      "buffer_load_dwordx4 v3, s[20:23], s40 offen offset:1024 lds\n\t"
      "v_mfma_f32_16x16x32_bf16 v[64:67], v[192:195], v[156:159], v[64:67]\n\t"
      "ds_read_b128 a[160:163], v18 offset:0\n\t"
      "v_mfma_f32_16x16x32_bf16 v[64:67], v[236:239], v[160:163], v[64:67]\n\t"
      "v_mfma_f32_16x16x32_bf16 a[8:11], v[240:243], v[128:131], a[8:11]\n\t"
      "v_mfma_f32_16x16x32_bf16 a[8:11], v[220:223], v[132:135], a[8:11]\n\t"
      "buffer_load_dwordx4 v4, s[20:23], s40 offen offset:2048 lds\n\t"
      "v_mfma_f32_16x16x32_bf16 a[40:43], v[240:243], v[132:135], a[40:43]\n\t"
      "ds_read_b128 a[164:167], v19 offset:0\n\t"
      "v_mfma_f32_16x16x32_bf16 a[40:43], v[220:223], v[136:139], a[40:43]\n\t"
      "v_mfma_f32_16x16x32_bf16 a[72:75], v[240:243], v[136:139], a[72:75]\n\t"
      "v_mfma_f32_16x16x32_bf16 a[72:75], v[220:223], v[140:143], a[72:75]\n\t"
      "buffer_load_dwordx4 v5, s[20:23], s40 offen offset:3072 lds\n\t"
      "v_mfma_f32_16x16x32_bf16 v[52:55], v[240:243], v[140:143], v[52:55]\n\t"
      "ds_read_b128 a[168:171], v20 offset:0\n\t"
      "v_mfma_f32_16x16x32_bf16 v[52:55], v[220:223], v[144:147], v[52:55]\n\t"
      "v_mfma_f32_16x16x32_bf16 a[24:27], v[240:243], v[144:147], a[24:27]\n\t"
      "s_add_u32 m0, s37, 13312\n\t"
      "s_mov_b32 s40, 13369344\n\t"
      "v_mfma_f32_16x16x32_bf16 a[24:27], v[220:223], v[148:151], a[24:27]\n\t"
      "s_waitcnt lgkmcnt(1)\n\t"
      "buffer_load_dwordx4 v2, s[20:23], s40 offen offset:0 lds\n\t"
      "v_mfma_f32_16x16x32_bf16 a[56:59], v[240:243], v[148:151], a[56:59]\n\t"
      "ds_read_b128 a[172:175], v21 offset:0\n\t"
      "v_mfma_f32_16x16x32_bf16 a[56:59], v[220:223], v[152:155], a[56:59]\n\t"
      "v_mfma_f32_16x16x32_bf16 a[88:91], v[240:243], v[152:155], a[88:91]\n\t"
      "s_waitcnt vmcnt(21)\n\t"
      "ds_read_b64_tr_b16 v[188:189], v6 offset:17408\n\t"
      "v_mfma_f32_16x16x32_bf16 a[88:91], v[220:223], v[156:159], a[88:91]\n\t"
      "ds_read_b64_tr_b16 v[190:191], v6 offset:17920\n\t"
      "v_mfma_f32_16x16x32_bf16 v[68:71], v[240:243], v[156:159], v[68:71]\n\t"
      "ds_read_b64_tr_b16 v[252:253], v7 offset:17408\n\t"
      "v_mfma_f32_16x16x32_bf16 v[68:71], v[220:223], v[160:163], v[68:71]\n\t"
      "ds_read_b64_tr_b16 v[254:255], v7 offset:17920\n\t"
      "v_mfma_f32_16x16x32_bf16 a[12:15], v[212:215], v[128:131], a[12:15]\n\t"
      "ds_read_b64_tr_b16 a[232:233], v8 offset:17408\n\t"
      "v_mfma_f32_16x16x32_bf16 a[12:15], v[180:183], v[132:135], a[12:15]\n\t"
      "ds_read_b64_tr_b16 a[234:235], v8 offset:17920\n\t"
      "v_mfma_f32_16x16x32_bf16 a[44:47], v[212:215], v[132:135], a[44:47]\n\t"
      "ds_read_b64_tr_b16 a[236:237], v9 offset:17408\n\t"
      "v_mfma_f32_16x16x32_bf16 a[44:47], v[180:183], v[136:139], a[44:47]\n\t"
      "ds_read_b64_tr_b16 a[238:239], v9 offset:17920\n\t"
      "v_mfma_f32_16x16x32_bf16 a[76:79], v[212:215], v[136:139], a[76:79]\n\t"
      "ds_read_b128 a[176:179], v18 offset:256\n\t"
      "v_mfma_f32_16x16x32_bf16 a[76:79], v[180:183], v[140:143], a[76:79]\n\t"
      "buffer_load_dwordx4 v3, s[20:23], s40 offen offset:1024 lds\n\t"
      "v_mfma_f32_16x16x32_bf16 v[56:59], v[212:215], v[140:143], v[56:59]\n\t"
      "v_mfma_f32_16x16x32_bf16 v[56:59], v[180:183], v[144:147], v[56:59]\n\t"
      "v_mfma_f32_16x16x32_bf16 a[28:31], v[212:215], v[144:147], a[28:31]\n\t"
      "ds_read_b128 a[180:183], v19 offset:256\n\t"
      "v_mfma_f32_16x16x32_bf16 a[28:31], v[180:183], v[148:151], a[28:31]\n\t"
      "buffer_load_dwordx4 v4, s[20:23], s40 offen offset:2048 lds\n\t"
      "v_mfma_f32_16x16x32_bf16 a[60:63], v[212:215], v[148:151], a[60:63]\n\t"
      "v_mfma_f32_16x16x32_bf16 a[60:63], v[180:183], v[152:155], a[60:63]\n\t"
      "v_mfma_f32_16x16x32_bf16 a[92:95], v[212:215], v[152:155], a[92:95]\n\t"
      "v_mfma_f32_16x16x32_bf16 a[92:95], v[180:183], v[156:159], a[92:95]\n\t"
      "buffer_load_dwordx4 v5, s[20:23], s40 offen offset:3072 lds\n\t"
      "v_mfma_f32_16x16x32_bf16 v[72:75], v[212:215], v[156:159], v[72:75]\n\t"
      "v_mfma_f32_16x16x32_bf16 v[72:75], v[180:183], v[160:163], v[72:75]\n\t"
      "v_mfma_f32_16x16x32_bf16 a[0:3], v[232:235], a[96:99], a[0:3]\n\t"
      "s_add_u32 m0, s37, 17408\n\t"
      "s_mov_b32 s40, 14417920\n\t"
      "v_mfma_f32_16x16x32_bf16 a[0:3], v[168:171], a[100:103], a[0:3]\n\t"
      "s_waitcnt lgkmcnt(0)\n\t"
      "buffer_load_dwordx4 v2, s[20:23], s40 offen offset:0 lds\n\t"
      "v_mfma_f32_16x16x32_bf16 a[32:35], v[232:235], a[100:103], a[32:35]\n\t"
      "s_waitcnt vmcnt(21)\n\t"
      "ds_read_b64_tr_b16 v[200:201], v6 offset:21504\n\t"
      "v_mfma_f32_16x16x32_bf16 a[32:35], v[168:171], a[104:107], a[32:35]\n\t"
      "ds_read_b64_tr_b16 v[202:203], v6 offset:22016\n\t"
      "v_mfma_f32_16x16x32_bf16 a[64:67], v[232:235], a[104:107], a[64:67]\n\t"
      "ds_read_b64_tr_b16 v[196:197], v7 offset:21504\n\t"
      "v_mfma_f32_16x16x32_bf16 a[64:67], v[168:171], a[108:111], a[64:67]\n\t"
      "ds_read_b64_tr_b16 v[198:199], v7 offset:22016\n\t"
      "v_mfma_f32_16x16x32_bf16 v[44:47], v[232:235], a[108:111], v[44:47]\n\t"
      "ds_read_b64_tr_b16 a[240:241], v8 offset:21504\n\t"
      "v_mfma_f32_16x16x32_bf16 v[44:47], v[168:171], a[112:115], v[44:47]\n\t"
      "ds_read_b64_tr_b16 a[242:243], v8 offset:22016\n\t"
      "v_mfma_f32_16x16x32_bf16 a[16:19], v[232:235], a[112:115], a[16:19]\n\t"
      "ds_read_b64_tr_b16 a[244:245], v9 offset:21504\n\t"
      "v_mfma_f32_16x16x32_bf16 a[16:19], v[168:171], a[116:119], a[16:19]\n\t"
      "ds_read_b64_tr_b16 a[246:247], v9 offset:22016\n\t"
      "v_mfma_f32_16x16x32_bf16 a[48:51], v[232:235], a[116:119], a[48:51]\n\t"
      "buffer_load_dwordx4 v3, s[20:23], s40 offen offset:1024 lds\n\t"
      "v_mfma_f32_16x16x32_bf16 a[48:51], v[168:171], a[120:123], a[48:51]\n\t"
      "v_mfma_f32_16x16x32_bf16 a[80:83], v[232:235], a[120:123], a[80:83]\n\t"
      "v_mfma_f32_16x16x32_bf16 a[80:83], v[168:171], a[124:127], a[80:83]\n\t"
      "v_mfma_f32_16x16x32_bf16 v[60:63], v[232:235], a[124:127], v[60:63]\n\t"
      "buffer_load_dwordx4 v4, s[20:23], s40 offen offset:2048 lds\n\t"
      "v_mfma_f32_16x16x32_bf16 v[60:63], v[168:171], a[128:131], v[60:63]\n\t"
      "v_mfma_f32_16x16x32_bf16 a[4:7], v[228:231], a[96:99], a[4:7]\n\t"
      "v_mfma_f32_16x16x32_bf16 a[4:7], v[244:247], a[100:103], a[4:7]\n\t"
      "v_mfma_f32_16x16x32_bf16 a[36:39], v[228:231], a[100:103], a[36:39]\n\t"
      "buffer_load_dwordx4 v5, s[20:23], s40 offen offset:3072 lds\n\t"
      "v_mfma_f32_16x16x32_bf16 a[36:39], v[244:247], a[104:107], a[36:39]\n\t"
      "v_mfma_f32_16x16x32_bf16 a[68:71], v[228:231], a[104:107], a[68:71]\n\t"
      "s_mov_b64 exec, 1\n\t"
      "buffer_load_dwordx2 v[38:39], off, s[28:31], s47 sc1\n\t"
      "s_mov_b64 exec, -1\n\t"
      "v_mfma_f32_16x16x32_bf16 a[68:71], v[244:247], a[108:111], a[68:71]\n\t"
      "s_add_u32 m0, s37, 21504\n\t"
      "s_mov_b32 s40, 15466496\n\t"
      "v_mfma_f32_16x16x32_bf16 v[48:51], v[228:231], a[108:111], v[48:51]\n\t"
      "s_waitcnt lgkmcnt(0)\n\t"
      "buffer_load_dwordx4 v2, s[20:23], s40 offen offset:0 lds\n\t"
      "v_mfma_f32_16x16x32_bf16 v[48:51], v[244:247], a[112:115], v[48:51]\n\t"
      "v_mfma_f32_16x16x32_bf16 a[20:23], v[228:231], a[112:115], a[20:23]\n\t"
      "s_waitcnt vmcnt(22)\n\t"
      "ds_read_b64_tr_b16 v[176:177], v6 offset:25600\n\t"
      "v_mfma_f32_16x16x32_bf16 a[20:23], v[244:247], a[116:119], a[20:23]\n\t"
      "ds_read_b64_tr_b16 v[178:179], v6 offset:26112\n\t"
      "v_mfma_f32_16x16x32_bf16 a[52:55], v[228:231], a[116:119], a[52:55]\n\t"
      "ds_read_b64_tr_b16 v[192:193], v7 offset:25600\n\t"
      "v_mfma_f32_16x16x32_bf16 a[52:55], v[244:247], a[120:123], a[52:55]\n\t"
      "ds_read_b64_tr_b16 v[194:195], v7 offset:26112\n\t"
      "v_mfma_f32_16x16x32_bf16 a[84:87], v[228:231], a[120:123], a[84:87]\n\t"
      "ds_read_b64_tr_b16 a[248:249], v8 offset:25600\n\t"
      "v_mfma_f32_16x16x32_bf16 a[84:87], v[244:247], a[124:127], a[84:87]\n\t"
      "ds_read_b64_tr_b16 a[250:251], v8 offset:26112\n\t"
      "v_mfma_f32_16x16x32_bf16 v[64:67], v[228:231], a[124:127], v[64:67]\n\t"
      "ds_read_b64_tr_b16 a[252:253], v9 offset:25600\n\t"
      "v_mfma_f32_16x16x32_bf16 v[64:67], v[244:247], a[128:131], v[64:67]\n\t"
      "ds_read_b64_tr_b16 a[254:255], v9 offset:26112\n\t"
      "v_mfma_f32_16x16x32_bf16 a[0:3], v[172:175], a[104:107], a[0:3]\n\t"
      "buffer_load_dwordx4 v3, s[20:23], s40 offen offset:1024 lds\n\t"
      "v_mfma_f32_16x16x32_bf16 a[0:3], v[208:211], a[108:111], a[0:3]\n\t"
      "v_mfma_f32_16x16x32_bf16 a[32:35], v[172:175], a[108:111], a[32:35]\n\t"
      "v_mfma_f32_16x16x32_bf16 a[32:35], v[208:211], a[112:115], a[32:35]\n\t"
      "v_mfma_f32_16x16x32_bf16 a[64:67], v[172:175], a[112:115], a[64:67]\n\t"
      "buffer_load_dwordx4 v4, s[20:23], s40 offen offset:2048 lds\n\t"
      "v_mfma_f32_16x16x32_bf16 a[64:67], v[208:211], a[116:119], a[64:67]\n\t"
      "v_mfma_f32_16x16x32_bf16 v[44:47], v[172:175], a[116:119], v[44:47]\n\t"
      "v_mfma_f32_16x16x32_bf16 v[44:47], v[208:211], a[120:123], v[44:47]\n\t"
      "v_mfma_f32_16x16x32_bf16 a[16:19], v[172:175], a[120:123], a[16:19]\n\t"
      "buffer_load_dwordx4 v5, s[20:23], s40 offen offset:3072 lds\n\t"
      "v_mfma_f32_16x16x32_bf16 a[16:19], v[208:211], a[124:127], a[16:19]\n\t"
      "v_mfma_f32_16x16x32_bf16 a[48:51], v[172:175], a[124:127], a[48:51]\n\t"
      "v_mfma_f32_16x16x32_bf16 a[48:51], v[208:211], a[128:131], a[48:51]\n\t"
      "v_mfma_f32_16x16x32_bf16 a[80:83], v[172:175], a[128:131], a[80:83]\n\t"
      "v_mfma_f32_16x16x32_bf16 a[80:83], v[208:211], a[132:135], a[80:83]\n\t"
      "v_mfma_f32_16x16x32_bf16 v[60:63], v[172:175], a[132:135], v[60:63]\n\t"
      "s_waitcnt vmcnt(21)\n\t"
      "ds_read_b64_tr_b16 v[236:237], v6 offset:29696\n\t"
      "v_mfma_f32_16x16x32_bf16 v[60:63], v[208:211], a[136:139], v[60:63]\n\t"
      "ds_read_b64_tr_b16 v[238:239], v6 offset:30208\n\t"
      "v_mfma_f32_16x16x32_bf16 a[4:7], v[224:227], a[104:107], a[4:7]\n\t"
      "ds_read_b64_tr_b16 v[240:241], v7 offset:29696\n\t"
      "v_mfma_f32_16x16x32_bf16 a[4:7], v[248:251], a[108:111], a[4:7]\n\t"
      "ds_read_b64_tr_b16 v[242:243], v7 offset:30208\n\t"
      "v_mfma_f32_16x16x32_bf16 a[36:39], v[224:227], a[108:111], a[36:39]\n\t"
      "ds_read_b64_tr_b16 v[220:221], v8 offset:29696\n\t"
      "v_mfma_f32_16x16x32_bf16 a[36:39], v[248:251], a[112:115], a[36:39]\n\t"
      "ds_read_b64_tr_b16 v[222:223], v8 offset:30208\n\t"
      "v_mfma_f32_16x16x32_bf16 a[68:71], v[224:227], a[112:115], a[68:71]\n\t"
      "ds_read_b64_tr_b16 v[212:213], v9 offset:29696\n\t"
      "v_mfma_f32_16x16x32_bf16 a[68:71], v[248:251], a[116:119], a[68:71]\n\t"
      "ds_read_b64_tr_b16 v[214:215], v9 offset:30208\n\t"
      "v_mfma_f32_16x16x32_bf16 v[48:51], v[224:227], a[116:119], v[48:51]\n\t"
      "v_mfma_f32_16x16x32_bf16 v[48:51], v[248:251], a[120:123], v[48:51]\n\t"
      "v_mfma_f32_16x16x32_bf16 a[20:23], v[224:227], a[120:123], a[20:23]\n\t"
      "v_mfma_f32_16x16x32_bf16 a[20:23], v[248:251], a[124:127], a[20:23]\n\t"
      "v_mfma_f32_16x16x32_bf16 a[52:55], v[224:227], a[124:127], a[52:55]\n\t"
      "v_mfma_f32_16x16x32_bf16 a[52:55], v[248:251], a[128:131], a[52:55]\n\t"
      "v_mfma_f32_16x16x32_bf16 a[84:87], v[224:227], a[128:131], a[84:87]\n\t"
      "v_mfma_f32_16x16x32_bf16 a[84:87], v[248:251], a[132:135], a[84:87]\n\t"
      "v_mfma_f32_16x16x32_bf16 v[64:67], v[224:227], a[132:135], v[64:67]\n\t"
      "v_mfma_f32_16x16x32_bf16 v[64:67], v[248:251], a[136:139], v[64:67]\n\t"
      "v_mfma_f32_16x16x32_bf16 a[0:3], v[184:187], a[112:115], a[0:3]\n\t"
      "v_mfma_f32_16x16x32_bf16 a[0:3], v[204:207], a[116:119], a[0:3]\n\t"
      "v_mfma_f32_16x16x32_bf16 a[32:35], v[184:187], a[116:119], a[32:35]\n\t"
      "v_mfma_f32_16x16x32_bf16 a[32:35], v[204:207], a[120:123], a[32:35]\n\t"
      "v_mfma_f32_16x16x32_bf16 a[64:67], v[184:187], a[120:123], a[64:67]\n\t"
      "s_waitcnt vmcnt(17)\n\t"
      "ds_read_b64_tr_b16 v[180:181], v6 offset:33792\n\t"
      "v_mfma_f32_16x16x32_bf16 a[64:67], v[204:207], a[124:127], a[64:67]\n\t"
      "ds_read_b64_tr_b16 v[182:183], v6 offset:34304\n\t"
      "v_mfma_f32_16x16x32_bf16 v[44:47], v[184:187], a[124:127], v[44:47]\n\t"
      "ds_read_b64_tr_b16 v[76:77], v7 offset:33792\n\t"
      "v_mfma_f32_16x16x32_bf16 v[44:47], v[204:207], a[128:131], v[44:47]\n\t"
      "ds_read_b64_tr_b16 v[78:79], v7 offset:34304\n\t"
      "v_mfma_f32_16x16x32_bf16 a[16:19], v[184:187], a[128:131], a[16:19]\n\t"
      "ds_read_b64_tr_b16 v[80:81], v8 offset:33792\n\t"
      "v_mfma_f32_16x16x32_bf16 a[16:19], v[204:207], a[132:135], a[16:19]\n\t"
      "ds_read_b64_tr_b16 v[82:83], v8 offset:34304\n\t"
      "v_mfma_f32_16x16x32_bf16 a[48:51], v[184:187], a[132:135], a[48:51]\n\t"
      "ds_read_b64_tr_b16 v[84:85], v9 offset:33792\n\t"
      "v_mfma_f32_16x16x32_bf16 a[48:51], v[204:207], a[136:139], a[48:51]\n\t"
      "ds_read_b64_tr_b16 v[86:87], v9 offset:34304\n\t"
      "v_mfma_f32_16x16x32_bf16 a[80:83], v[184:187], a[136:139], a[80:83]\n\t"
      "v_mfma_f32_16x16x32_bf16 a[80:83], v[204:207], a[140:143], a[80:83]\n\t"
      "v_mfma_f32_16x16x32_bf16 v[60:63], v[184:187], a[140:143], v[60:63]\n\t"
      "v_mfma_f32_16x16x32_bf16 v[60:63], v[204:207], a[144:147], v[60:63]\n\t"
      "v_mfma_f32_16x16x32_bf16 a[4:7], v[164:167], a[112:115], a[4:7]\n\t"
      "v_mfma_f32_16x16x32_bf16 a[4:7], v[216:219], a[116:119], a[4:7]\n\t"
      "v_mfma_f32_16x16x32_bf16 a[36:39], v[164:167], a[116:119], a[36:39]\n\t"
      "v_mfma_f32_16x16x32_bf16 a[36:39], v[216:219], a[120:123], a[36:39]\n\t"
      "v_mfma_f32_16x16x32_bf16 a[68:71], v[164:167], a[120:123], a[68:71]\n\t"
      "v_mfma_f32_16x16x32_bf16 a[68:71], v[216:219], a[124:127], a[68:71]\n\t"
      "v_mfma_f32_16x16x32_bf16 v[48:51], v[164:167], a[124:127], v[48:51]\n\t"
      "v_mfma_f32_16x16x32_bf16 v[48:51], v[216:219], a[128:131], v[48:51]\n\t"
      "v_mfma_f32_16x16x32_bf16 a[20:23], v[164:167], a[128:131], a[20:23]\n\t"
      "v_mfma_f32_16x16x32_bf16 a[20:23], v[216:219], a[132:135], a[20:23]\n\t"
      "v_mfma_f32_16x16x32_bf16 a[52:55], v[164:167], a[132:135], a[52:55]\n\t"
      "s_waitcnt vmcnt(13)\n\t"
      "ds_read_b64_tr_b16 v[88:89], v6 offset:9216\n\t"
      "v_mfma_f32_16x16x32_bf16 a[52:55], v[216:219], a[136:139], a[52:55]\n\t"
      "ds_read_b64_tr_b16 v[90:91], v6 offset:9728\n\t"
      "v_mfma_f32_16x16x32_bf16 a[84:87], v[164:167], a[136:139], a[84:87]\n\t"
      "ds_read_b64_tr_b16 v[92:93], v7 offset:9216\n\t"
      "v_mfma_f32_16x16x32_bf16 a[84:87], v[216:219], a[140:143], a[84:87]\n\t"
      "ds_read_b64_tr_b16 v[94:95], v7 offset:9728\n\t"
      "v_mfma_f32_16x16x32_bf16 v[64:67], v[164:167], a[140:143], v[64:67]\n\t"
      "ds_read_b64_tr_b16 v[96:97], v8 offset:9216\n\t"
      "v_mfma_f32_16x16x32_bf16 v[64:67], v[216:219], a[144:147], v[64:67]\n\t"
      "ds_read_b64_tr_b16 v[98:99], v8 offset:9728\n\t"
      "v_mfma_f32_16x16x32_bf16 a[0:3], v[188:191], a[120:123], a[0:3]\n\t"
      "ds_read_b64_tr_b16 v[100:101], v9 offset:9216\n\t"
      "v_mfma_f32_16x16x32_bf16 a[0:3], v[200:203], a[124:127], a[0:3]\n\t"
      "ds_read_b64_tr_b16 v[102:103], v9 offset:9728\n\t"
      "v_mfma_f32_16x16x32_bf16 a[32:35], v[188:191], a[124:127], a[32:35]\n\t"
      "v_mfma_f32_16x16x32_bf16 a[32:35], v[200:203], a[128:131], a[32:35]\n\t"
      "v_mfma_f32_16x16x32_bf16 a[64:67], v[188:191], a[128:131], a[64:67]\n\t"
      "v_mfma_f32_16x16x32_bf16 a[64:67], v[200:203], a[132:135], a[64:67]\n\t"
      "v_mfma_f32_16x16x32_bf16 a[16:19], v[188:191], a[136:139], a[16:19]\n\t"
      "v_mfma_f32_16x16x32_bf16 a[16:19], v[200:203], a[140:143], a[16:19]\n\t"
      "v_mfma_f32_16x16x32_bf16 a[48:51], v[188:191], a[140:143], a[48:51]\n\t"
      "v_mfma_f32_16x16x32_bf16 a[48:51], v[200:203], a[144:147], a[48:51]\n\t"
      "v_mfma_f32_16x16x32_bf16 a[80:83], v[188:191], a[144:147], a[80:83]\n\t"
      "v_mfma_f32_16x16x32_bf16 a[80:83], v[200:203], a[148:151], a[80:83]\n\t"
      "v_mfma_f32_16x16x32_bf16 a[4:7], v[252:255], a[120:123], a[4:7]\n\t"
      "v_mfma_f32_16x16x32_bf16 a[4:7], v[196:199], a[124:127], a[4:7]\n\t"
      "v_mfma_f32_16x16x32_bf16 a[36:39], v[252:255], a[124:127], a[36:39]\n\t"
      "v_mfma_f32_16x16x32_bf16 a[36:39], v[196:199], a[128:131], a[36:39]\n\t"
      "v_mfma_f32_16x16x32_bf16 a[68:71], v[252:255], a[128:131], a[68:71]\n\t"
      "s_waitcnt vmcnt(9)\n\t"
      "ds_read_b64_tr_b16 v[104:105], v6 offset:13312\n\t"
      "v_mfma_f32_16x16x32_bf16 a[68:71], v[196:199], a[132:135], a[68:71]\n\t"
      "ds_read_b64_tr_b16 v[106:107], v6 offset:13824\n\t"
      "v_mfma_f32_16x16x32_bf16 a[20:23], v[252:255], a[136:139], a[20:23]\n\t"
      "ds_read_b64_tr_b16 v[108:109], v7 offset:13312\n\t"
      "v_mfma_f32_16x16x32_bf16 a[20:23], v[196:199], a[140:143], a[20:23]\n\t"
      "ds_read_b64_tr_b16 v[110:111], v7 offset:13824\n\t"
      "v_mfma_f32_16x16x32_bf16 a[52:55], v[252:255], a[140:143], a[52:55]\n\t"
      "ds_read_b64_tr_b16 v[112:113], v8 offset:13312\n\t"
      "v_mfma_f32_16x16x32_bf16 a[52:55], v[196:199], a[144:147], a[52:55]\n\t"
      "ds_read_b64_tr_b16 v[114:115], v8 offset:13824\n\t"
      "v_mfma_f32_16x16x32_bf16 a[84:87], v[252:255], a[144:147], a[84:87]\n\t"
      "ds_read_b64_tr_b16 v[116:117], v9 offset:13312\n\t"
      "v_mfma_f32_16x16x32_bf16 a[84:87], v[196:199], a[148:151], a[84:87]\n\t"
      "ds_read_b64_tr_b16 v[118:119], v9 offset:13824\n\t"
      "s_waitcnt lgkmcnt(5)\n\t"
      "v_mfma_f32_16x16x32_bf16 a[0:3], v[176:179], a[128:131], a[0:3]\n\t"
      "s_mov_b64 exec, 1\n\t"
      "s_waitcnt vmcnt(4)\n\t"
      "v_cmp_ne_u64 vcc, v[38:39], v[36:37]\n\t"
      "s_cbranch_vccnz Lhs%=\n\t"
      "Lhsb%=:\n\t"
      "s_mov_b64 exec, -1\n\t"
      "v_mfma_f32_16x16x32_bf16 a[0:3], v[236:239], a[132:135], a[0:3]\n\t"
      "v_mfma_f32_16x16x32_bf16 a[32:35], v[176:179], a[132:135], a[32:35]\n\t"
      "v_mfma_f32_16x16x32_bf16 a[32:35], v[236:239], a[136:139], a[32:35]\n\t"
      "v_mfma_f32_16x16x32_bf16 a[64:67], v[176:179], a[136:139], a[64:67]\n\t"
      "v_mfma_f32_16x16x32_bf16 a[64:67], v[236:239], a[140:143], a[64:67]\n\t"
      "v_mfma_f32_16x16x32_bf16 a[16:19], v[176:179], a[144:147], a[16:19]\n\t"
      "v_mfma_f32_16x16x32_bf16 a[16:19], v[236:239], a[148:151], a[16:19]\n\t"
      "v_mfma_f32_16x16x32_bf16 a[48:51], v[176:179], a[148:151], a[48:51]\n\t"
      "v_mfma_f32_16x16x32_bf16 a[48:51], v[236:239], a[152:155], a[48:51]\n\t"
      "v_mfma_f32_16x16x32_bf16 a[80:83], v[176:179], a[152:155], a[80:83]\n\t"
      "v_mfma_f32_16x16x32_bf16 a[80:83], v[236:239], a[156:159], a[80:83]\n\t"
      "v_mfma_f32_16x16x32_bf16 a[4:7], v[192:195], a[128:131], a[4:7]\n\t"
      "v_mfma_f32_16x16x32_bf16 a[4:7], v[240:243], a[132:135], a[4:7]\n\t"
      "v_mfma_f32_16x16x32_bf16 a[36:39], v[192:195], a[132:135], a[36:39]\n\t"
      "ds_read_b64_tr_b16 v[120:121], v6 offset:17408\n\t"
      "v_mfma_f32_16x16x32_bf16 a[36:39], v[240:243], a[136:139], a[36:39]\n\t"
      "ds_read_b64_tr_b16 v[122:123], v6 offset:17920\n\t"
      "v_mfma_f32_16x16x32_bf16 a[68:71], v[192:195], a[136:139], a[68:71]\n\t"
      "ds_read_b64_tr_b16 v[124:125], v7 offset:17408\n\t"
      "v_mfma_f32_16x16x32_bf16 a[68:71], v[240:243], a[140:143], a[68:71]\n\t"
      "ds_read_b64_tr_b16 v[126:127], v7 offset:17920\n\t"
      "v_mfma_f32_16x16x32_bf16 a[20:23], v[192:195], a[144:147], a[20:23]\n\t"
      "ds_read_b64_tr_b16 v[128:129], v8 offset:17408\n\t"
      "v_mfma_f32_16x16x32_bf16 a[20:23], v[240:243], a[148:151], a[20:23]\n\t"
      "ds_read_b64_tr_b16 v[130:131], v8 offset:17920\n\t"
      "v_mfma_f32_16x16x32_bf16 a[52:55], v[192:195], a[148:151], a[52:55]\n\t"
      "ds_read_b64_tr_b16 v[132:133], v9 offset:17408\n\t"
      "v_mfma_f32_16x16x32_bf16 a[52:55], v[240:243], a[152:155], a[52:55]\n\t"
      "ds_read_b64_tr_b16 v[134:135], v9 offset:17920\n\t"
      "v_mfma_f32_16x16x32_bf16 a[84:87], v[192:195], a[152:155], a[84:87]\n\t"
      "v_mfma_f32_16x16x32_bf16 a[84:87], v[240:243], a[156:159], a[84:87]\n\t"
      "v_mfma_f32_16x16x32_bf16 a[0:3], v[180:183], a[136:139], a[0:3]\n\t"
      "v_mfma_f32_16x16x32_bf16 a[0:3], v[88:91], a[140:143], a[0:3]\n\t"
      "v_mfma_f32_16x16x32_bf16 a[32:35], v[180:183], a[140:143], a[32:35]\n\t"
      "v_mfma_f32_16x16x32_bf16 a[32:35], v[88:91], a[144:147], a[32:35]\n\t"
      "v_mfma_f32_16x16x32_bf16 a[64:67], v[180:183], a[144:147], a[64:67]\n\t"
      "v_mfma_f32_16x16x32_bf16 a[64:67], v[88:91], a[148:151], a[64:67]\n\t"
      "v_mfma_f32_16x16x32_bf16 a[16:19], v[180:183], a[152:155], a[16:19]\n\t"
      "v_mfma_f32_16x16x32_bf16 a[16:19], v[88:91], a[156:159], a[16:19]\n\t"
      "v_mfma_f32_16x16x32_bf16 a[48:51], v[180:183], a[156:159], a[48:51]\n\t"
      "v_mfma_f32_16x16x32_bf16 a[48:51], v[88:91], a[160:163], a[48:51]\n\t"
      "v_mfma_f32_16x16x32_bf16 a[80:83], v[180:183], a[160:163], a[80:83]\n\t"
      "v_mfma_f32_16x16x32_bf16 a[80:83], v[88:91], a[164:167], a[80:83]\n\t"
      "v_mfma_f32_16x16x32_bf16 a[4:7], v[76:79], a[136:139], a[4:7]\n\t"
      "s_waitcnt vmcnt(0)\n\t"
      "ds_read_b64_tr_b16 v[136:137], v6 offset:21504\n\t"
      "v_mfma_f32_16x16x32_bf16 a[4:7], v[92:95], a[140:143], a[4:7]\n\t"
      "ds_read_b64_tr_b16 v[138:139], v6 offset:22016\n\t"
      "v_mfma_f32_16x16x32_bf16 a[36:39], v[76:79], a[140:143], a[36:39]\n\t"
      "ds_read_b64_tr_b16 v[140:141], v7 offset:21504\n\t"
      "v_mfma_f32_16x16x32_bf16 a[36:39], v[92:95], a[144:147], a[36:39]\n\t"
      "ds_read_b64_tr_b16 v[142:143], v7 offset:22016\n\t"
      "v_mfma_f32_16x16x32_bf16 a[68:71], v[76:79], a[144:147], a[68:71]\n\t"
      "ds_read_b64_tr_b16 v[144:145], v8 offset:21504\n\t"
      "v_mfma_f32_16x16x32_bf16 a[68:71], v[92:95], a[148:151], a[68:71]\n\t"
      "ds_read_b64_tr_b16 v[146:147], v8 offset:22016\n\t"
      "v_mfma_f32_16x16x32_bf16 a[20:23], v[76:79], a[152:155], a[20:23]\n\t"
      "ds_read_b64_tr_b16 v[148:149], v9 offset:21504\n\t"
      "v_mfma_f32_16x16x32_bf16 a[20:23], v[92:95], a[156:159], a[20:23]\n\t"
      "ds_read_b64_tr_b16 v[150:151], v9 offset:22016\n\t"
      "v_mfma_f32_16x16x32_bf16 a[52:55], v[76:79], a[156:159], a[52:55]\n\t"
      "v_mfma_f32_16x16x32_bf16 a[52:55], v[92:95], a[160:163], a[52:55]\n\t"
      "v_mfma_f32_16x16x32_bf16 a[84:87], v[76:79], a[160:163], a[84:87]\n\t"
      "v_mfma_f32_16x16x32_bf16 a[84:87], v[92:95], a[164:167], a[84:87]\n\t"
      "v_mfma_f32_16x16x32_bf16 a[0:3], v[104:107], a[144:147], a[0:3]\n\t"
      "s_waitcnt lgkmcnt(0)\n\t"
      "v_mfma_f32_16x16x32_bf16 a[0:3], v[120:123], a[148:151], a[0:3]\n\t"
      "v_mfma_f32_16x16x32_bf16 a[0:3], v[136:139], a[152:155], a[0:3]\n\t"
      "v_mfma_f32_16x16x32_bf16 a[4:7], v[108:111], a[144:147], a[4:7]\n\t"
      "v_mfma_f32_16x16x32_bf16 a[4:7], v[124:127], a[148:151], a[4:7]\n\t"
      "v_mfma_f32_16x16x32_bf16 a[4:7], v[140:143], a[152:155], a[4:7]\n\t"
      "v_mfma_f32_16x16x32_bf16 a[16:19], v[104:107], a[160:163], a[16:19]\n\t"
      "v_mfma_f32_16x16x32_bf16 a[16:19], v[120:123], a[164:167], a[16:19]\n\t"
      "v_mfma_f32_16x16x32_bf16 a[16:19], v[136:139], a[168:171], a[16:19]\n\t"
      "v_mfma_f32_16x16x32_bf16 a[20:23], v[108:111], a[160:163], a[20:23]\n\t"
      "s_nop 0\n\t"
      "ds_write_b128 v32, a[0:3] offset:0\n\t"
      "v_mfma_f32_16x16x32_bf16 a[20:23], v[124:127], a[164:167], a[20:23]\n\t"
      "v_mfma_f32_16x16x32_bf16 a[20:23], v[140:143], a[168:171], a[20:23]\n\t"
      "v_mfma_f32_16x16x32_bf16 a[32:35], v[104:107], a[148:151], a[32:35]\n\t"
      "ds_write_b128 v32, a[4:7] offset:1024\n\t"
      "v_mfma_f32_16x16x32_bf16 a[32:35], v[120:123], a[152:155], a[32:35]\n\t"
      "v_mfma_f32_16x16x32_bf16 a[32:35], v[136:139], a[156:159], a[32:35]\n\t"
      "v_mfma_f32_16x16x32_bf16 a[36:39], v[108:111], a[148:151], a[36:39]\n\t"
      "ds_write_b128 v32, a[16:19] offset:2048\n\t"
      "v_mfma_f32_16x16x32_bf16 a[36:39], v[124:127], a[152:155], a[36:39]\n\t"
      "v_mfma_f32_16x16x32_bf16 a[36:39], v[140:143], a[156:159], a[36:39]\n\t"
      "v_mfma_f32_16x16x32_bf16 a[48:51], v[104:107], a[164:167], a[48:51]\n\t"
      "ds_write_b128 v32, a[20:23] offset:3072\n\t"
      "v_mfma_f32_16x16x32_bf16 a[48:51], v[120:123], a[168:171], a[48:51]\n\t"
      "v_mfma_f32_16x16x32_bf16 a[48:51], v[136:139], a[172:175], a[48:51]\n\t"
      "v_mfma_f32_16x16x32_bf16 a[52:55], v[108:111], a[164:167], a[52:55]\n\t"
      "ds_write_b128 v32, a[32:35] offset:4096\n\t"
      "v_mfma_f32_16x16x32_bf16 a[52:55], v[124:127], a[168:171], a[52:55]\n\t"
      "v_mfma_f32_16x16x32_bf16 a[52:55], v[140:143], a[172:175], a[52:55]\n\t"
      "v_mfma_f32_16x16x32_bf16 a[64:67], v[104:107], a[152:155], a[64:67]\n\t"
      "ds_write_b128 v32, a[36:39] offset:5120\n\t"
      "v_mfma_f32_16x16x32_bf16 a[64:67], v[120:123], a[156:159], a[64:67]\n\t"
      "v_mfma_f32_16x16x32_bf16 a[64:67], v[136:139], a[160:163], a[64:67]\n\t"
      "v_mfma_f32_16x16x32_bf16 a[68:71], v[108:111], a[152:155], a[68:71]\n\t"
      "ds_write_b128 v32, a[48:51] offset:6144\n\t"
      "v_mfma_f32_16x16x32_bf16 a[68:71], v[124:127], a[156:159], a[68:71]\n\t"
      "v_mfma_f32_16x16x32_bf16 a[68:71], v[140:143], a[160:163], a[68:71]\n\t"
      "v_mfma_f32_16x16x32_bf16 a[80:83], v[104:107], a[168:171], a[80:83]\n\t"
      "ds_write_b128 v32, a[52:55] offset:7168\n\t"
      "v_mfma_f32_16x16x32_bf16 a[80:83], v[120:123], a[172:175], a[80:83]\n\t"
      "v_mfma_f32_16x16x32_bf16 a[80:83], v[136:139], a[176:179], a[80:83]\n\t"
      "v_mfma_f32_16x16x32_bf16 a[84:87], v[108:111], a[168:171], a[84:87]\n\t"
      "ds_write_b128 v32, a[64:67] offset:8192\n\t"
      "v_mfma_f32_16x16x32_bf16 a[84:87], v[124:127], a[172:175], a[84:87]\n\t"
      "v_mfma_f32_16x16x32_bf16 a[84:87], v[140:143], a[176:179], a[84:87]\n\t"
      "v_mfma_f32_16x16x32_bf16 v[44:47], v[188:191], a[132:135], v[44:47]\n\t"
      "ds_write_b128 v32, a[68:71] offset:9216\n\t"
      "v_mfma_f32_16x16x32_bf16 v[44:47], v[200:203], a[136:139], v[44:47]\n\t"
      "v_mfma_f32_16x16x32_bf16 v[44:47], v[176:179], a[140:143], v[44:47]\n\t"
      "v_mfma_f32_16x16x32_bf16 v[44:47], v[236:239], a[144:147], v[44:47]\n\t"
      "ds_write_b128 v32, a[80:83] offset:10240\n\t"
      "v_mfma_f32_16x16x32_bf16 v[44:47], v[180:183], a[148:151], v[44:47]\n\t"
      "v_mfma_f32_16x16x32_bf16 v[44:47], v[88:91], a[152:155], v[44:47]\n\t"
      "v_mfma_f32_16x16x32_bf16 v[44:47], v[104:107], a[156:159], v[44:47]\n\t"
      "ds_write_b128 v32, a[84:87] offset:11264\n\t"
      "v_mfma_f32_16x16x32_bf16 v[44:47], v[120:123], a[160:163], v[44:47]\n\t"
      "v_mfma_f32_16x16x32_bf16 v[44:47], v[136:139], a[164:167], v[44:47]\n\t"
      "v_mfma_f32_16x16x32_bf16 v[60:63], v[188:191], a[148:151], v[60:63]\n\t"
      "v_mfma_f32_16x16x32_bf16 v[60:63], v[200:203], a[152:155], v[60:63]\n\t"
      "s_waitcnt lgkmcnt(0)\n\t"
      "s_barrier\n\t"
      "v_mfma_f32_16x16x32_bf16 v[60:63], v[176:179], a[156:159], v[60:63]\n\t"
      "ds_read_b128 v[152:155], v22 offset:0\n\t"
      "ds_read_b128 v[156:159], v24 offset:4096\n\t"
      "v_mfma_f32_16x16x32_bf16 v[60:63], v[236:239], a[160:163], v[60:63]\n\t"
      "ds_read_b128 v[160:163], v26 offset:8192\n\t"
      "ds_read_b128 v[232:235], v23 offset:0\n\t"
      "v_mfma_f32_16x16x32_bf16 v[60:63], v[180:183], a[164:167], v[60:63]\n\t"
      "ds_read_b128 v[168:171], v25 offset:4096\n\t"
      "ds_read_b128 v[228:231], v27 offset:8192\n\t"
      "v_mfma_f32_16x16x32_bf16 v[60:63], v[88:91], a[168:171], v[60:63]\n\t"
      "ds_read_b128 v[244:247], v22 offset:1024\n\t"
      "ds_read_b128 v[172:175], v24 offset:5120\n\t"
      "v_mfma_f32_16x16x32_bf16 v[60:63], v[104:107], a[172:175], v[60:63]\n\t"
      "ds_read_b128 v[208:211], v26 offset:9216\n\t"
      "ds_read_b128 v[224:227], v23 offset:1024\n\t"
      "v_mfma_f32_16x16x32_bf16 v[60:63], v[120:123], a[176:179], v[60:63]\n\t"
      "ds_read_b128 v[248:251], v25 offset:5120\n\t"
      "ds_read_b128 v[184:187], v27 offset:9216\n\t"
      "v_mfma_f32_16x16x32_bf16 v[60:63], v[136:139], a[180:183], v[60:63]\n\t"
      "v_mfma_f32_16x16x32_bf16 v[48:51], v[252:255], a[132:135], v[48:51]\n\t"
      "v_mfma_f32_16x16x32_bf16 v[48:51], v[196:199], a[136:139], v[48:51]\n\t"
      "v_mfma_f32_16x16x32_bf16 v[48:51], v[192:195], a[140:143], v[48:51]\n\t"
      "s_waitcnt lgkmcnt(2)\n\t"
      "v_add_f32 v152, v152, v156\n\t"
      "v_add_f32 v153, v153, v157\n\t"
      "v_mfma_f32_16x16x32_bf16 v[48:51], v[240:243], a[144:147], v[48:51]\n\t"
      "v_add_f32 v154, v154, v158\n\t"
      "v_add_f32 v155, v155, v159\n\t"
      "v_mfma_f32_16x16x32_bf16 v[48:51], v[76:79], a[148:151], v[48:51]\n\t"
      "v_add_f32 v152, v152, v160\n\t"
      "v_add_f32 v153, v153, v161\n\t"
      "v_mfma_f32_16x16x32_bf16 v[48:51], v[92:95], a[152:155], v[48:51]\n\t"
      "v_add_f32 v154, v154, v162\n\t"
      "v_add_f32 v155, v155, v163\n\t"
      "v_mfma_f32_16x16x32_bf16 v[48:51], v[108:111], a[156:159], v[48:51]\n\t"
      "v_mul_f32 v152, s48, v152\n\t"
      "v_mul_f32 v153, s48, v153\n\t"
      "v_mfma_f32_16x16x32_bf16 v[48:51], v[124:127], a[160:163], v[48:51]\n\t"
      "v_mul_f32 v154, s48, v154\n\t"
      "v_mul_f32 v155, s48, v155\n\t"
      "v_mfma_f32_16x16x32_bf16 v[48:51], v[140:143], a[164:167], v[48:51]\n\t"
      "v_fma_f32 v152, v44, s48, v152\n\t"
      "v_fma_f32 v153, v45, s48, v153\n\t"
      "v_mfma_f32_16x16x32_bf16 v[64:67], v[252:255], a[148:151], v[64:67]\n\t"
      "v_fma_f32 v154, v46, s48, v154\n\t"
      "v_fma_f32 v155, v47, s48, v155\n\t"
      "v_mfma_f32_16x16x32_bf16 v[64:67], v[196:199], a[152:155], v[64:67]\n\t"
      "v_exp_f32 v152, v152\n\t"
      "v_mfma_f32_16x16x32_bf16 v[64:67], v[192:195], a[156:159], v[64:67]\n\t"
      "v_exp_f32 v153, v153\n\t"
      "v_mfma_f32_16x16x32_bf16 v[64:67], v[240:243], a[160:163], v[64:67]\n\t"
      "v_exp_f32 v154, v154\n\t"
      "v_mfma_f32_16x16x32_bf16 v[64:67], v[76:79], a[164:167], v[64:67]\n\t"
      "v_exp_f32 v155, v155\n\t"
      "v_mfma_f32_16x16x32_bf16 v[64:67], v[92:95], a[168:171], v[64:67]\n\t"
      "v_add_f32 v152, 1.0, v152\n\t"
      "v_add_f32 v153, 1.0, v153\n\t"
      "v_mfma_f32_16x16x32_bf16 v[64:67], v[108:111], a[172:175], v[64:67]\n\t"
      "v_add_f32 v154, 1.0, v154\n\t"
      "v_add_f32 v155, 1.0, v155\n\t"
      "v_mfma_f32_16x16x32_bf16 v[64:67], v[124:127], a[176:179], v[64:67]\n\t"
      "v_rcp_f32 v152, v152\n\t"
      "v_mfma_f32_16x16x32_bf16 v[64:67], v[140:143], a[180:183], v[64:67]\n\t"
      "v_rcp_f32 v153, v153\n\t"
      "v_mfma_f32_16x16x32_bf16 a[8:11], a[184:187], a[96:99], a[8:11]\n\t"
      "v_rcp_f32 v154, v154\n\t"
      "v_mfma_f32_16x16x32_bf16 a[8:11], a[192:195], a[100:103], a[8:11]\n\t"
      "v_rcp_f32 v155, v155\n\t"
      "v_mfma_f32_16x16x32_bf16 a[8:11], a[200:203], a[104:107], a[8:11]\n\t"
      "v_fma_mixlo_f16 v156, v152, s49, v41\n\t"
      "v_fma_mixlo_f16 v157, v154, s49, v41\n\t"
      "v_mfma_f32_16x16x32_bf16 a[8:11], a[208:211], a[108:111], a[8:11]\n\t"
      "v_fma_mixhi_f16 v156, v153, s49, v41\n\t"
      "v_fma_mixhi_f16 v157, v155, s49, v41\n\t"
      "v_mfma_f32_16x16x32_bf16 a[8:11], a[216:219], a[112:115], a[8:11]\n\t"
      "v_add_f32 v232, v232, v168\n\t"
      "v_add_f32 v233, v233, v169\n\t"
      "v_mfma_f32_16x16x32_bf16 a[8:11], a[224:227], a[116:119], a[8:11]\n\t"
      "v_add_f32 v234, v234, v170\n\t"
      "v_add_f32 v235, v235, v171\n\t"
      "v_mfma_f32_16x16x32_bf16 a[8:11], a[232:235], a[120:123], a[8:11]\n\t"
      "v_add_f32 v232, v232, v228\n\t"
      "v_add_f32 v233, v233, v229\n\t"
      "v_mfma_f32_16x16x32_bf16 a[8:11], a[240:243], a[124:127], a[8:11]\n\t"
      "v_add_f32 v234, v234, v230\n\t"
      "v_add_f32 v235, v235, v231\n\t"
      "v_mfma_f32_16x16x32_bf16 a[8:11], a[248:251], a[128:131], a[8:11]\n\t"
      "v_mul_f32 v232, s48, v232\n\t"
      "v_mul_f32 v233, s48, v233\n\t"
      "v_mfma_f32_16x16x32_bf16 a[8:11], v[220:223], a[132:135], a[8:11]\n\t"
      "v_mul_f32 v234, s48, v234\n\t"
      "v_mul_f32 v235, s48, v235\n\t"
      "v_mfma_f32_16x16x32_bf16 a[8:11], v[80:83], a[136:139], a[8:11]\n\t"
      "v_fma_f32 v232, v60, s48, v232\n\t"
      "v_fma_f32 v233, v61, s48, v233\n\t"
      "v_mfma_f32_16x16x32_bf16 a[8:11], v[96:99], a[140:143], a[8:11]\n\t"
      "v_fma_f32 v234, v62, s48, v234\n\t"
      "v_fma_f32 v235, v63, s48, v235\n\t"
      "v_mfma_f32_16x16x32_bf16 a[8:11], v[112:115], a[144:147], a[8:11]\n\t"
      "v_exp_f32 v232, v232\n\t"
      "v_mfma_f32_16x16x32_bf16 a[8:11], v[128:131], a[148:151], a[8:11]\n\t"
      "v_exp_f32 v233, v233\n\t"
      "v_mfma_f32_16x16x32_bf16 a[8:11], v[144:147], a[152:155], a[8:11]\n\t"
      "v_exp_f32 v234, v234\n\t"
      "v_mfma_f32_16x16x32_bf16 a[24:27], a[184:187], a[112:115], a[24:27]\n\t"
      "v_exp_f32 v235, v235\n\t"
      "v_mfma_f32_16x16x32_bf16 a[24:27], a[192:195], a[116:119], a[24:27]\n\t"
      "v_add_f32 v232, 1.0, v232\n\t"
      "v_add_f32 v233, 1.0, v233\n\t"
      "v_mfma_f32_16x16x32_bf16 a[24:27], a[200:203], a[120:123], a[24:27]\n\t"
      "v_add_f32 v234, 1.0, v234\n\t"
      "v_add_f32 v235, 1.0, v235\n\t"
      "v_mfma_f32_16x16x32_bf16 a[24:27], a[208:211], a[124:127], a[24:27]\n\t"
      "v_rcp_f32 v232, v232\n\t"
      "v_mfma_f32_16x16x32_bf16 a[24:27], a[216:219], a[128:131], a[24:27]\n\t"
      "v_rcp_f32 v233, v233\n\t"
      "v_mfma_f32_16x16x32_bf16 a[24:27], a[224:227], a[132:135], a[24:27]\n\t"
      "v_rcp_f32 v234, v234\n\t"
      "v_mfma_f32_16x16x32_bf16 a[24:27], a[232:235], a[136:139], a[24:27]\n\t"
      "ds_write_b128 v32, a[8:11] offset:12288\n\t"
      "v_mfma_f32_16x16x32_bf16 a[24:27], a[240:243], a[140:143], a[24:27]\n\t"
      "v_rcp_f32 v235, v235\n\t"
      "v_mfma_f32_16x16x32_bf16 a[24:27], a[248:251], a[144:147], a[24:27]\n\t"
      "v_fma_mixlo_f16 v158, v232, s49, v41\n\t"
      "v_fma_mixlo_f16 v159, v234, s49, v41\n\t"
      "v_mfma_f32_16x16x32_bf16 a[24:27], v[220:223], a[148:151], a[24:27]\n\t"
      "v_fma_mixhi_f16 v158, v233, s49, v41\n\t"
      "v_fma_mixhi_f16 v159, v235, s49, v41\n\t"
      "v_mfma_f32_16x16x32_bf16 a[24:27], v[80:83], a[152:155], a[24:27]\n\t"
      "buffer_store_dwordx4 v[156:159], v31, s[28:31], s45 offen offset:0 sc1\n\t"
      "v_add_f32 v244, v244, v172\n\t"
      "v_mfma_f32_16x16x32_bf16 a[24:27], v[96:99], a[156:159], a[24:27]\n\t"
      "v_add_f32 v245, v245, v173\n\t"
      "v_add_f32 v246, v246, v174\n\t"
      "v_mfma_f32_16x16x32_bf16 a[24:27], v[112:115], a[160:163], a[24:27]\n\t"
      "v_add_f32 v247, v247, v175\n\t"
      "v_add_f32 v244, v244, v208\n\t"
      "v_mfma_f32_16x16x32_bf16 a[24:27], v[128:131], a[164:167], a[24:27]\n\t"
      "v_add_f32 v245, v245, v209\n\t"
      "v_add_f32 v246, v246, v210\n\t"
      "v_mfma_f32_16x16x32_bf16 a[24:27], v[144:147], a[168:171], a[24:27]\n\t"
      "v_add_f32 v247, v247, v211\n\t"
      "v_mul_f32 v244, s48, v244\n\t"
      "v_mfma_f32_16x16x32_bf16 a[40:43], a[184:187], a[100:103], a[40:43]\n\t"
      "v_mul_f32 v245, s48, v245\n\t"
      "v_mul_f32 v246, s48, v246\n\t"
      "v_mfma_f32_16x16x32_bf16 a[40:43], a[192:195], a[104:107], a[40:43]\n\t"
      "v_mul_f32 v247, s48, v247\n\t"
      "v_fma_f32 v244, v48, s48, v244\n\t"
      "v_mfma_f32_16x16x32_bf16 a[40:43], a[200:203], a[108:111], a[40:43]\n\t"
      "v_fma_f32 v245, v49, s48, v245\n\t"
      "v_fma_f32 v246, v50, s48, v246\n\t"
      "v_mfma_f32_16x16x32_bf16 a[40:43], a[208:211], a[112:115], a[40:43]\n\t"
      "v_fma_f32 v247, v51, s48, v247\n\t"
      "v_mfma_f32_16x16x32_bf16 a[40:43], a[216:219], a[116:119], a[40:43]\n\t"
      "v_exp_f32 v244, v244\n\t"
      "v_mfma_f32_16x16x32_bf16 a[40:43], a[224:227], a[120:123], a[40:43]\n\t"
      "v_exp_f32 v245, v245\n\t"
      "v_mfma_f32_16x16x32_bf16 a[40:43], a[232:235], a[124:127], a[40:43]\n\t"
      "ds_write_b128 v32, a[24:27] offset:14336\n\t"
      "v_mfma_f32_16x16x32_bf16 a[40:43], a[240:243], a[128:131], a[40:43]\n\t"
      "v_exp_f32 v246, v246\n\t"
      "v_mfma_f32_16x16x32_bf16 a[40:43], a[248:251], a[132:135], a[40:43]\n\t"
      "v_exp_f32 v247, v247\n\t"
      "v_mfma_f32_16x16x32_bf16 a[40:43], v[220:223], a[136:139], a[40:43]\n\t"
      "v_add_f32 v244, 1.0, v244\n\t"
      "v_add_f32 v245, 1.0, v245\n\t"
      "v_mfma_f32_16x16x32_bf16 a[40:43], v[80:83], a[140:143], a[40:43]\n\t"
      "v_add_f32 v246, 1.0, v246\n\t"
      "v_add_f32 v247, 1.0, v247\n\t"
      "v_mfma_f32_16x16x32_bf16 a[40:43], v[96:99], a[144:147], a[40:43]\n\t"
      "v_rcp_f32 v244, v244\n\t"
      "v_mfma_f32_16x16x32_bf16 a[40:43], v[112:115], a[148:151], a[40:43]\n\t"
      "v_rcp_f32 v245, v245\n\t"
      "v_mfma_f32_16x16x32_bf16 a[40:43], v[128:131], a[152:155], a[40:43]\n\t"
      "v_rcp_f32 v246, v246\n\t"
      "v_mfma_f32_16x16x32_bf16 a[40:43], v[144:147], a[156:159], a[40:43]\n\t"
      "v_rcp_f32 v247, v247\n\t"
      "v_mfma_f32_16x16x32_bf16 a[56:59], a[184:187], a[116:119], a[56:59]\n\t"
      "v_fma_mixlo_f16 v172, v244, s49, v41\n\t"
      "v_fma_mixlo_f16 v173, v246, s49, v41\n\t"
      "v_mfma_f32_16x16x32_bf16 a[56:59], a[192:195], a[120:123], a[56:59]\n\t"
      "v_fma_mixhi_f16 v172, v245, s49, v41\n\t"
      "v_fma_mixhi_f16 v173, v247, s49, v41\n\t"
      "v_mfma_f32_16x16x32_bf16 a[56:59], a[200:203], a[124:127], a[56:59]\n\t"
      "s_waitcnt lgkmcnt(0)\n\t"
      "v_add_f32 v224, v224, v248\n\t"
      "v_add_f32 v225, v225, v249\n\t"
      "v_mfma_f32_16x16x32_bf16 a[56:59], a[208:211], a[128:131], a[56:59]\n\t"
      "v_add_f32 v226, v226, v250\n\t"
      "v_add_f32 v227, v227, v251\n\t"
      "v_mfma_f32_16x16x32_bf16 a[56:59], a[216:219], a[132:135], a[56:59]\n\t"
      "v_add_f32 v224, v224, v184\n\t"
      "v_add_f32 v225, v225, v185\n\t"
      "v_mfma_f32_16x16x32_bf16 a[56:59], a[224:227], a[136:139], a[56:59]\n\t"
      "v_add_f32 v226, v226, v186\n\t"
      "v_add_f32 v227, v227, v187\n\t"
      "v_mfma_f32_16x16x32_bf16 a[56:59], a[232:235], a[140:143], a[56:59]\n\t"
      "ds_write_b128 v32, a[40:43] offset:16384\n\t"
      "v_mul_f32 v224, s48, v224\n\t"
      "v_mfma_f32_16x16x32_bf16 a[56:59], a[240:243], a[144:147], a[56:59]\n\t"
      "v_mul_f32 v225, s48, v225\n\t"
      "v_mul_f32 v226, s48, v226\n\t"
      "v_mfma_f32_16x16x32_bf16 a[56:59], a[248:251], a[148:151], a[56:59]\n\t"
      "v_mul_f32 v227, s48, v227\n\t"
      "v_fma_f32 v224, v64, s48, v224\n\t"
      "v_mfma_f32_16x16x32_bf16 a[56:59], v[220:223], a[152:155], a[56:59]\n\t"
      "v_fma_f32 v225, v65, s48, v225\n\t"
      "v_fma_f32 v226, v66, s48, v226\n\t"
      "v_mfma_f32_16x16x32_bf16 a[56:59], v[80:83], a[156:159], a[56:59]\n\t"
      "v_fma_f32 v227, v67, s48, v227\n\t"
      "v_mfma_f32_16x16x32_bf16 a[56:59], v[96:99], a[160:163], a[56:59]\n\t"
      "v_exp_f32 v224, v224\n\t"
      "v_mfma_f32_16x16x32_bf16 a[56:59], v[112:115], a[164:167], a[56:59]\n\t"
      "v_exp_f32 v225, v225\n\t"
      "v_mfma_f32_16x16x32_bf16 a[56:59], v[128:131], a[168:171], a[56:59]\n\t"
      "v_exp_f32 v226, v226\n\t"
      "v_mfma_f32_16x16x32_bf16 a[56:59], v[144:147], a[172:175], a[56:59]\n\t"
      "v_exp_f32 v227, v227\n\t"
      "v_mfma_f32_16x16x32_bf16 a[72:75], a[184:187], a[104:107], a[72:75]\n\t"
      "v_add_f32 v224, 1.0, v224\n\t"
      "v_add_f32 v225, 1.0, v225\n\t"
      "v_mfma_f32_16x16x32_bf16 a[72:75], a[192:195], a[108:111], a[72:75]\n\t"
      "v_add_f32 v226, 1.0, v226\n\t"
      "v_add_f32 v227, 1.0, v227\n\t"
      "v_mfma_f32_16x16x32_bf16 a[72:75], a[200:203], a[112:115], a[72:75]\n\t"
      "v_rcp_f32 v224, v224\n\t"
      "v_mfma_f32_16x16x32_bf16 a[72:75], a[208:211], a[116:119], a[72:75]\n\t"
      "v_rcp_f32 v225, v225\n\t"
      "v_mfma_f32_16x16x32_bf16 a[72:75], a[216:219], a[120:123], a[72:75]\n\t"
      "v_rcp_f32 v226, v226\n\t"
      "v_mfma_f32_16x16x32_bf16 a[72:75], a[224:227], a[124:127], a[72:75]\n\t"
      "v_rcp_f32 v227, v227\n\t"
      "v_mfma_f32_16x16x32_bf16 a[72:75], a[232:235], a[128:131], a[72:75]\n\t"
      "ds_write_b128 v32, a[56:59] offset:18432\n\t"
      "v_fma_mixlo_f16 v174, v224, s49, v41\n\t"
      "v_mfma_f32_16x16x32_bf16 a[72:75], a[240:243], a[132:135], a[72:75]\n\t"
      "v_fma_mixlo_f16 v175, v226, s49, v41\n\t"
      "v_fma_mixhi_f16 v174, v225, s49, v41\n\t"
      "v_mfma_f32_16x16x32_bf16 a[72:75], a[248:251], a[136:139], a[72:75]\n\t"
      "v_fma_mixhi_f16 v175, v227, s49, v41\n\t"
      "s_nop 0\n\t"
      "buffer_store_dwordx4 v[172:175], v31, s[28:31], s45 offen offset:1024 sc1\n\t"
      "v_mfma_f32_16x16x32_bf16 a[72:75], v[220:223], a[140:143], a[72:75]\n\t"
      "v_mfma_f32_16x16x32_bf16 a[72:75], v[80:83], a[144:147], a[72:75]\n\t"
      "v_mfma_f32_16x16x32_bf16 a[72:75], v[96:99], a[148:151], a[72:75]\n\t"
      "v_mfma_f32_16x16x32_bf16 a[72:75], v[112:115], a[152:155], a[72:75]\n\t"
      "v_mfma_f32_16x16x32_bf16 a[72:75], v[128:131], a[156:159], a[72:75]\n\t"
      "v_mfma_f32_16x16x32_bf16 a[72:75], v[144:147], a[160:163], a[72:75]\n\t"
      "v_mfma_f32_16x16x32_bf16 a[88:91], a[184:187], a[120:123], a[88:91]\n\t"
      "v_mfma_f32_16x16x32_bf16 a[88:91], a[192:195], a[124:127], a[88:91]\n\t"
      "v_mfma_f32_16x16x32_bf16 a[88:91], a[200:203], a[128:131], a[88:91]\n\t"
      "v_mfma_f32_16x16x32_bf16 a[88:91], a[208:211], a[132:135], a[88:91]\n\t"
      "v_mfma_f32_16x16x32_bf16 a[88:91], a[216:219], a[136:139], a[88:91]\n\t"
      "v_mfma_f32_16x16x32_bf16 a[88:91], a[224:227], a[140:143], a[88:91]\n\t"
      "v_mfma_f32_16x16x32_bf16 a[88:91], a[232:235], a[144:147], a[88:91]\n\t"
      "s_nop 0\n\t"
      "ds_write_b128 v32, a[72:75] offset:20480\n\t"
      "v_mfma_f32_16x16x32_bf16 a[88:91], a[240:243], a[148:151], a[88:91]\n\t"
      "v_mfma_f32_16x16x32_bf16 a[88:91], a[248:251], a[152:155], a[88:91]\n\t"
      "v_mfma_f32_16x16x32_bf16 a[88:91], v[220:223], a[156:159], a[88:91]\n\t"
      "v_mfma_f32_16x16x32_bf16 a[88:91], v[80:83], a[160:163], a[88:91]\n\t"
      "v_mfma_f32_16x16x32_bf16 a[88:91], v[96:99], a[164:167], a[88:91]\n\t"
      "v_mfma_f32_16x16x32_bf16 a[88:91], v[112:115], a[168:171], a[88:91]\n\t"
      "v_mfma_f32_16x16x32_bf16 a[88:91], v[128:131], a[172:175], a[88:91]\n\t"
      "v_mfma_f32_16x16x32_bf16 a[88:91], v[144:147], a[176:179], a[88:91]\n\t"
      "v_mfma_f32_16x16x32_bf16 a[12:15], a[188:191], a[96:99], a[12:15]\n\t"
      "v_mfma_f32_16x16x32_bf16 a[12:15], a[196:199], a[100:103], a[12:15]\n\t"
      "v_mfma_f32_16x16x32_bf16 a[12:15], a[204:207], a[104:107], a[12:15]\n\t"
      "v_mfma_f32_16x16x32_bf16 a[12:15], a[212:215], a[108:111], a[12:15]\n\t"
      "v_mfma_f32_16x16x32_bf16 a[12:15], a[220:223], a[112:115], a[12:15]\n\t"
      "v_mfma_f32_16x16x32_bf16 a[12:15], a[228:231], a[116:119], a[12:15]\n\t"
      "v_mfma_f32_16x16x32_bf16 a[12:15], a[236:239], a[120:123], a[12:15]\n\t"
      "s_nop 0\n\t"
      "ds_write_b128 v32, a[88:91] offset:22528\n\t"
      "v_mfma_f32_16x16x32_bf16 a[12:15], a[244:247], a[124:127], a[12:15]\n\t"
      "v_mfma_f32_16x16x32_bf16 a[12:15], a[252:255], a[128:131], a[12:15]\n\t"
      "v_mfma_f32_16x16x32_bf16 a[12:15], v[212:215], a[132:135], a[12:15]\n\t"
      "v_mfma_f32_16x16x32_bf16 a[12:15], v[84:87], a[136:139], a[12:15]\n\t"
      "s_waitcnt lgkmcnt(0)\n\t"
      "s_barrier\n\t"
      "v_mfma_f32_16x16x32_bf16 a[12:15], v[100:103], a[140:143], a[12:15]\n\t"
      "ds_read_b128 v[152:155], v22 offset:12288\n\t"
      "ds_read_b128 v[156:159], v24 offset:16384\n\t"
      "v_mfma_f32_16x16x32_bf16 a[12:15], v[116:119], a[144:147], a[12:15]\n\t"
      "ds_read_b128 v[160:163], v26 offset:20480\n\t"
      "ds_read_b128 v[232:235], v23 offset:12288\n\t"
      "v_mfma_f32_16x16x32_bf16 a[12:15], v[132:135], a[148:151], a[12:15]\n\t"
      "ds_read_b128 v[168:171], v25 offset:16384\n\t"
      "ds_read_b128 v[228:231], v27 offset:20480\n\t"
      "v_mfma_f32_16x16x32_bf16 a[12:15], v[148:151], a[152:155], a[12:15]\n\t"
      "v_mfma_f32_16x16x32_bf16 a[28:31], a[188:191], a[112:115], a[28:31]\n\t"
      "v_mfma_f32_16x16x32_bf16 a[28:31], a[196:199], a[116:119], a[28:31]\n\t"
      "v_mfma_f32_16x16x32_bf16 a[28:31], a[204:207], a[120:123], a[28:31]\n\t"
      "v_mfma_f32_16x16x32_bf16 a[28:31], a[212:215], a[124:127], a[28:31]\n\t"
      "v_mfma_f32_16x16x32_bf16 a[28:31], a[220:223], a[128:131], a[28:31]\n\t"
      "v_mfma_f32_16x16x32_bf16 a[28:31], a[228:231], a[132:135], a[28:31]\n\t"
      "s_waitcnt lgkmcnt(0)\n\t"
      "v_add_f32 v152, v152, v156\n\t"
      "v_add_f32 v153, v153, v157\n\t"
      "v_mfma_f32_16x16x32_bf16 a[28:31], a[236:239], a[136:139], a[28:31]\n\t"
      "ds_write_b128 v32, a[12:15] offset:13312\n\t"
      "v_add_f32 v154, v154, v158\n\t"
      "v_mfma_f32_16x16x32_bf16 a[28:31], a[244:247], a[140:143], a[28:31]\n\t"
      "v_add_f32 v155, v155, v159\n\t"
      "v_add_f32 v152, v152, v160\n\t"
      "v_mfma_f32_16x16x32_bf16 a[28:31], a[252:255], a[144:147], a[28:31]\n\t"
      "v_add_f32 v153, v153, v161\n\t"
      "v_add_f32 v154, v154, v162\n\t"
      "v_mfma_f32_16x16x32_bf16 a[28:31], v[212:215], a[148:151], a[28:31]\n\t"
      "v_add_f32 v155, v155, v163\n\t"
      "v_mul_f32 v152, s48, v152\n\t"
      "v_mfma_f32_16x16x32_bf16 a[28:31], v[84:87], a[152:155], a[28:31]\n\t"
      "v_mul_f32 v153, s48, v153\n\t"
      "v_mul_f32 v154, s48, v154\n\t"
      "v_mfma_f32_16x16x32_bf16 a[28:31], v[100:103], a[156:159], a[28:31]\n\t"
      "v_mul_f32 v155, s48, v155\n\t"
      "v_add_f32 v232, v232, v168\n\t"
      "v_mfma_f32_16x16x32_bf16 a[28:31], v[116:119], a[160:163], a[28:31]\n\t"
      "v_add_f32 v233, v233, v169\n\t"
      "v_add_f32 v234, v234, v170\n\t"
      "v_mfma_f32_16x16x32_bf16 a[28:31], v[132:135], a[164:167], a[28:31]\n\t"
      "v_add_f32 v235, v235, v171\n\t"
      "v_add_f32 v232, v232, v228\n\t"
      "v_mfma_f32_16x16x32_bf16 a[28:31], v[148:151], a[168:171], a[28:31]\n\t"
      "v_add_f32 v233, v233, v229\n\t"
      "v_add_f32 v234, v234, v230\n\t"
      "v_mfma_f32_16x16x32_bf16 a[44:47], a[188:191], a[100:103], a[44:47]\n\t"
      "v_add_f32 v235, v235, v231\n\t"
      "v_mul_f32 v232, s48, v232\n\t"
      "v_mfma_f32_16x16x32_bf16 a[44:47], a[196:199], a[104:107], a[44:47]\n\t"
      "v_mul_f32 v233, s48, v233\n\t"
      "v_mul_f32 v234, s48, v234\n\t"
      "v_mfma_f32_16x16x32_bf16 a[44:47], a[204:207], a[108:111], a[44:47]\n\t"
      "v_mul_f32 v235, s48, v235\n\t"
      "v_mfma_f32_16x16x32_bf16 a[44:47], a[212:215], a[112:115], a[44:47]\n\t"
      "v_mfma_f32_16x16x32_bf16 a[44:47], a[220:223], a[116:119], a[44:47]\n\t"
      "v_mfma_f32_16x16x32_bf16 a[44:47], a[228:231], a[120:123], a[44:47]\n\t"
      "v_mfma_f32_16x16x32_bf16 a[44:47], a[236:239], a[124:127], a[44:47]\n\t"
      "ds_write_b128 v32, a[28:31] offset:15360\n\t"
      "v_mfma_f32_16x16x32_bf16 a[44:47], a[244:247], a[128:131], a[44:47]\n\t"
      "v_mfma_f32_16x16x32_bf16 a[44:47], a[252:255], a[132:135], a[44:47]\n\t"
      "v_mfma_f32_16x16x32_bf16 a[44:47], v[212:215], a[136:139], a[44:47]\n\t"
      "v_mfma_f32_16x16x32_bf16 a[44:47], v[84:87], a[140:143], a[44:47]\n\t"
      "v_mfma_f32_16x16x32_bf16 a[44:47], v[100:103], a[144:147], a[44:47]\n\t"
      "v_mfma_f32_16x16x32_bf16 a[44:47], v[116:119], a[148:151], a[44:47]\n\t"
      "v_mfma_f32_16x16x32_bf16 a[44:47], v[132:135], a[152:155], a[44:47]\n\t"
      "v_mfma_f32_16x16x32_bf16 a[44:47], v[148:151], a[156:159], a[44:47]\n\t"
      "v_mfma_f32_16x16x32_bf16 a[60:63], a[188:191], a[116:119], a[60:63]\n\t"
      "v_mfma_f32_16x16x32_bf16 a[60:63], a[196:199], a[120:123], a[60:63]\n\t"
      "v_mfma_f32_16x16x32_bf16 a[60:63], a[204:207], a[124:127], a[60:63]\n\t"
      "v_mfma_f32_16x16x32_bf16 a[60:63], a[212:215], a[128:131], a[60:63]\n\t"
      "v_mfma_f32_16x16x32_bf16 a[60:63], a[220:223], a[132:135], a[60:63]\n\t"
      "v_mfma_f32_16x16x32_bf16 a[60:63], a[228:231], a[136:139], a[60:63]\n\t"
      "v_mfma_f32_16x16x32_bf16 a[60:63], a[236:239], a[140:143], a[60:63]\n\t"
      "s_nop 0\n\t"
      "ds_write_b128 v32, a[44:47] offset:17408\n\t"
      "v_mfma_f32_16x16x32_bf16 a[60:63], a[244:247], a[144:147], a[60:63]\n\t"
      "v_mfma_f32_16x16x32_bf16 a[60:63], a[252:255], a[148:151], a[60:63]\n\t"
      "v_mfma_f32_16x16x32_bf16 a[60:63], v[212:215], a[152:155], a[60:63]\n\t"
      "v_mfma_f32_16x16x32_bf16 a[60:63], v[84:87], a[156:159], a[60:63]\n\t"
      "v_mfma_f32_16x16x32_bf16 a[60:63], v[100:103], a[160:163], a[60:63]\n\t"
      "v_mfma_f32_16x16x32_bf16 a[60:63], v[116:119], a[164:167], a[60:63]\n\t"
      "v_mfma_f32_16x16x32_bf16 a[60:63], v[132:135], a[168:171], a[60:63]\n\t"
      "v_mfma_f32_16x16x32_bf16 a[60:63], v[148:151], a[172:175], a[60:63]\n\t"
      "v_mfma_f32_16x16x32_bf16 a[76:79], a[188:191], a[104:107], a[76:79]\n\t"
      "v_mfma_f32_16x16x32_bf16 a[76:79], a[196:199], a[108:111], a[76:79]\n\t"
      "v_mfma_f32_16x16x32_bf16 a[76:79], a[204:207], a[112:115], a[76:79]\n\t"
      "v_mfma_f32_16x16x32_bf16 a[76:79], a[212:215], a[116:119], a[76:79]\n\t"
      "v_mfma_f32_16x16x32_bf16 a[76:79], a[220:223], a[120:123], a[76:79]\n\t"
      "v_mfma_f32_16x16x32_bf16 a[76:79], a[228:231], a[124:127], a[76:79]\n\t"
      "v_mfma_f32_16x16x32_bf16 a[76:79], a[236:239], a[128:131], a[76:79]\n\t"
      "s_nop 0\n\t"
      "ds_write_b128 v32, a[60:63] offset:19456\n\t"
      "v_mfma_f32_16x16x32_bf16 a[76:79], a[244:247], a[132:135], a[76:79]\n\t"
      "v_mfma_f32_16x16x32_bf16 a[76:79], a[252:255], a[136:139], a[76:79]\n\t"
      "v_mfma_f32_16x16x32_bf16 a[76:79], v[212:215], a[140:143], a[76:79]\n\t"
      "v_mfma_f32_16x16x32_bf16 a[76:79], v[84:87], a[144:147], a[76:79]\n\t"
      "v_mfma_f32_16x16x32_bf16 a[76:79], v[100:103], a[148:151], a[76:79]\n\t"
      "v_mfma_f32_16x16x32_bf16 a[76:79], v[116:119], a[152:155], a[76:79]\n\t"
      "v_mfma_f32_16x16x32_bf16 a[76:79], v[132:135], a[156:159], a[76:79]\n\t"
      "v_mfma_f32_16x16x32_bf16 a[76:79], v[148:151], a[160:163], a[76:79]\n\t"
      "v_mfma_f32_16x16x32_bf16 a[92:95], a[188:191], a[120:123], a[92:95]\n\t"
      "v_mfma_f32_16x16x32_bf16 a[92:95], a[196:199], a[124:127], a[92:95]\n\t"
      "v_mfma_f32_16x16x32_bf16 a[92:95], a[204:207], a[128:131], a[92:95]\n\t"
      "v_mfma_f32_16x16x32_bf16 a[92:95], a[212:215], a[132:135], a[92:95]\n\t"
      "v_mfma_f32_16x16x32_bf16 a[92:95], a[220:223], a[136:139], a[92:95]\n\t"
      "v_mfma_f32_16x16x32_bf16 a[92:95], a[228:231], a[140:143], a[92:95]\n\t"
      "v_mfma_f32_16x16x32_bf16 a[92:95], a[236:239], a[144:147], a[92:95]\n\t"
      "s_nop 0\n\t"
      "ds_write_b128 v32, a[76:79] offset:21504\n\t"
      "v_mfma_f32_16x16x32_bf16 a[92:95], a[244:247], a[148:151], a[92:95]\n\t"
      "v_mfma_f32_16x16x32_bf16 a[92:95], a[252:255], a[152:155], a[92:95]\n\t"
      "v_mfma_f32_16x16x32_bf16 a[92:95], v[212:215], a[156:159], a[92:95]\n\t"
      "v_mfma_f32_16x16x32_bf16 a[92:95], v[84:87], a[160:163], a[92:95]\n\t"
      "buffer_load_dwordx4 v[204:207], v31, s[28:31], s44 offen offset:0 sc1\n\t"
      "v_mfma_f32_16x16x32_bf16 a[92:95], v[100:103], a[164:167], a[92:95]\n\t"
      "buffer_load_dwordx4 v[164:167], v31, s[28:31], s44 offen offset:1024 sc1\n\t"
      "v_mfma_f32_16x16x32_bf16 a[92:95], v[116:119], a[168:171], a[92:95]\n\t"
      "v_mfma_f32_16x16x32_bf16 a[92:95], v[132:135], a[172:175], a[92:95]\n\t"
      "v_mfma_f32_16x16x32_bf16 a[92:95], v[148:151], a[176:179], a[92:95]\n\t"
      "v_mfma_f32_16x16x32_bf16 v[52:55], a[184:187], a[108:111], v[52:55]\n\t"
      "v_mfma_f32_16x16x32_bf16 v[52:55], a[192:195], a[112:115], v[52:55]\n\t"
      "v_mfma_f32_16x16x32_bf16 v[52:55], a[200:203], a[116:119], v[52:55]\n\t"
      "v_mfma_f32_16x16x32_bf16 v[52:55], a[208:211], a[120:123], v[52:55]\n\t"
      "v_mfma_f32_16x16x32_bf16 v[52:55], a[216:219], a[124:127], v[52:55]\n\t"
      "v_mfma_f32_16x16x32_bf16 v[52:55], a[224:227], a[128:131], v[52:55]\n\t"
      "v_mfma_f32_16x16x32_bf16 v[52:55], a[232:235], a[132:135], v[52:55]\n\t"
      "s_nop 0\n\t"
      "ds_write_b128 v32, a[92:95] offset:23552\n\t"
      "v_mfma_f32_16x16x32_bf16 v[52:55], a[240:243], a[136:139], v[52:55]\n\t"
      "v_mfma_f32_16x16x32_bf16 v[52:55], a[248:251], a[140:143], v[52:55]\n\t"
      "v_mfma_f32_16x16x32_bf16 v[52:55], v[220:223], a[144:147], v[52:55]\n\t"
      "v_mfma_f32_16x16x32_bf16 v[52:55], v[80:83], a[148:151], v[52:55]\n\t"
      "s_waitcnt lgkmcnt(0)\n\t"
      "s_barrier\n\t"
      "v_mfma_f32_16x16x32_bf16 v[52:55], v[96:99], a[152:155], v[52:55]\n\t"
      "ds_read_b128 v[244:247], v22 offset:13312\n\t"
      "ds_read_b128 v[172:175], v24 offset:17408\n\t"
      "v_mfma_f32_16x16x32_bf16 v[52:55], v[112:115], a[156:159], v[52:55]\n\t"
      "ds_read_b128 v[208:211], v26 offset:21504\n\t"
      "ds_read_b128 v[224:227], v23 offset:13312\n\t"
      "v_mfma_f32_16x16x32_bf16 v[52:55], v[128:131], a[160:163], v[52:55]\n\t"
      "ds_read_b128 v[248:251], v25 offset:17408\n\t"
      "ds_read_b128 v[184:187], v27 offset:21504\n\t"
      "v_mfma_f32_16x16x32_bf16 v[52:55], v[144:147], a[164:167], v[52:55]\n\t"
      "v_mfma_f32_16x16x32_bf16 v[68:71], a[184:187], a[124:127], v[68:71]\n\t"
      "v_mfma_f32_16x16x32_bf16 v[68:71], a[192:195], a[128:131], v[68:71]\n\t"
      "v_mfma_f32_16x16x32_bf16 v[68:71], a[200:203], a[132:135], v[68:71]\n\t"
      "v_mfma_f32_16x16x32_bf16 v[68:71], a[208:211], a[136:139], v[68:71]\n\t"
      "v_mfma_f32_16x16x32_bf16 v[68:71], a[216:219], a[140:143], v[68:71]\n\t"
      "v_mfma_f32_16x16x32_bf16 v[68:71], a[224:227], a[144:147], v[68:71]\n\t"
      "s_waitcnt lgkmcnt(0)\n\t"
      "v_add_f32 v244, v244, v172\n\t"
      "v_add_f32 v245, v245, v173\n\t"
      "v_mfma_f32_16x16x32_bf16 v[68:71], a[232:235], a[148:151], v[68:71]\n\t"
      "v_add_f32 v246, v246, v174\n\t"
      "v_add_f32 v247, v247, v175\n\t"
      "v_mfma_f32_16x16x32_bf16 v[68:71], a[240:243], a[152:155], v[68:71]\n\t"
      "v_add_f32 v244, v244, v208\n\t"
      "v_add_f32 v245, v245, v209\n\t"
      "v_mfma_f32_16x16x32_bf16 v[68:71], a[248:251], a[156:159], v[68:71]\n\t"
      "v_add_f32 v246, v246, v210\n\t"
      "v_add_f32 v247, v247, v211\n\t"
      "v_mfma_f32_16x16x32_bf16 v[68:71], v[220:223], a[160:163], v[68:71]\n\t"
      "v_mul_f32 v244, s48, v244\n\t"
      "v_mul_f32 v245, s48, v245\n\t"
      "v_mfma_f32_16x16x32_bf16 v[68:71], v[80:83], a[164:167], v[68:71]\n\t"
      "v_mul_f32 v246, s48, v246\n\t"
      "v_mul_f32 v247, s48, v247\n\t"
      "v_mfma_f32_16x16x32_bf16 v[68:71], v[96:99], a[168:171], v[68:71]\n\t"
      "v_add_f32 v224, v224, v248\n\t"
      "v_add_f32 v225, v225, v249\n\t"
      "v_mfma_f32_16x16x32_bf16 v[68:71], v[112:115], a[172:175], v[68:71]\n\t"
      "v_add_f32 v226, v226, v250\n\t"
      "v_add_f32 v227, v227, v251\n\t"
      "v_mfma_f32_16x16x32_bf16 v[68:71], v[128:131], a[176:179], v[68:71]\n\t"
      "v_add_f32 v224, v224, v184\n\t"
      "v_add_f32 v225, v225, v185\n\t"
      "v_mfma_f32_16x16x32_bf16 v[68:71], v[144:147], a[180:183], v[68:71]\n\t"
      "v_add_f32 v226, v226, v186\n\t"
      "v_add_f32 v227, v227, v187\n\t"
      "v_mfma_f32_16x16x32_bf16 v[56:59], a[188:191], a[108:111], v[56:59]\n\t"
      "v_mul_f32 v224, s48, v224\n\t"
      "v_mul_f32 v225, s48, v225\n\t"
      "v_mfma_f32_16x16x32_bf16 v[56:59], a[196:199], a[112:115], v[56:59]\n\t"
      "v_mul_f32 v226, s48, v226\n\t"
      "v_mul_f32 v227, s48, v227\n\t"
      "v_mfma_f32_16x16x32_bf16 v[56:59], a[204:207], a[116:119], v[56:59]\n\t"
      "v_fma_f32 v152, v52, s48, v152\n\t"
      "v_fma_f32 v153, v53, s48, v153\n\t"
      "v_mfma_f32_16x16x32_bf16 v[56:59], a[212:215], a[120:123], v[56:59]\n\t"
      "v_fma_f32 v154, v54, s48, v154\n\t"
      "v_fma_f32 v155, v55, s48, v155\n\t"
      "v_mfma_f32_16x16x32_bf16 v[56:59], a[220:223], a[124:127], v[56:59]\n\t"
      "v_exp_f32 v152, v152\n\t"
      "v_mfma_f32_16x16x32_bf16 v[56:59], a[228:231], a[128:131], v[56:59]\n\t"
      "v_exp_f32 v153, v153\n\t"
      "v_mfma_f32_16x16x32_bf16 v[56:59], a[236:239], a[132:135], v[56:59]\n\t"
      "v_exp_f32 v154, v154\n\t"
      "v_mfma_f32_16x16x32_bf16 v[56:59], a[244:247], a[136:139], v[56:59]\n\t"
      "v_exp_f32 v155, v155\n\t"
      "v_mfma_f32_16x16x32_bf16 v[56:59], a[252:255], a[140:143], v[56:59]\n\t"
      "v_add_f32 v152, 1.0, v152\n\t"
      "v_add_f32 v153, 1.0, v153\n\t"
      "v_mfma_f32_16x16x32_bf16 v[56:59], v[212:215], a[144:147], v[56:59]\n\t"
      "v_add_f32 v154, 1.0, v154\n\t"
      "v_add_f32 v155, 1.0, v155\n\t"
      "v_mfma_f32_16x16x32_bf16 v[56:59], v[84:87], a[148:151], v[56:59]\n\t"
      "v_rcp_f32 v152, v152\n\t"
      "v_mfma_f32_16x16x32_bf16 v[56:59], v[100:103], a[152:155], v[56:59]\n\t"
      "v_rcp_f32 v153, v153\n\t"
      "v_mfma_f32_16x16x32_bf16 v[56:59], v[116:119], a[156:159], v[56:59]\n\t"
      "v_rcp_f32 v154, v154\n\t"
      "v_mfma_f32_16x16x32_bf16 v[56:59], v[132:135], a[160:163], v[56:59]\n\t"
      "v_rcp_f32 v155, v155\n\t"
      "v_mfma_f32_16x16x32_bf16 v[56:59], v[148:151], a[164:167], v[56:59]\n\t"
      "v_fma_f32 v152, v152, s8, v40\n\t"
      "v_fma_f32 v153, v153, s8, v40\n\t"
      "v_mfma_f32_16x16x32_bf16 v[72:75], a[188:191], a[124:127], v[72:75]\n\t"
      "v_fma_f32 v154, v154, s8, v40\n\t"
      "v_fma_f32 v155, v155, s8, v40\n\t"
      "v_mfma_f32_16x16x32_bf16 v[72:75], a[196:199], a[128:131], v[72:75]\n\t"
      "v_fma_f32 v232, v68, s48, v232\n\t"
      "v_fma_f32 v233, v69, s48, v233\n\t"
      "v_mfma_f32_16x16x32_bf16 v[72:75], a[204:207], a[132:135], v[72:75]\n\t"
      "v_fma_f32 v234, v70, s48, v234\n\t"
      "v_fma_f32 v235, v71, s48, v235\n\t"
      "v_mfma_f32_16x16x32_bf16 v[72:75], a[212:215], a[136:139], v[72:75]\n\t"
      "v_exp_f32 v232, v232\n\t"
      "v_mfma_f32_16x16x32_bf16 v[72:75], a[220:223], a[140:143], v[72:75]\n\t"
      "v_exp_f32 v233, v233\n\t"
      "v_mfma_f32_16x16x32_bf16 v[72:75], a[228:231], a[144:147], v[72:75]\n\t"
      "v_exp_f32 v234, v234\n\t"
      "v_mfma_f32_16x16x32_bf16 v[72:75], a[236:239], a[148:151], v[72:75]\n\t"
      "v_exp_f32 v235, v235\n\t"
      "v_mfma_f32_16x16x32_bf16 v[72:75], a[244:247], a[152:155], v[72:75]\n\t"
      "v_add_f32 v232, 1.0, v232\n\t"
      "v_add_f32 v233, 1.0, v233\n\t"
      "v_mfma_f32_16x16x32_bf16 v[72:75], a[252:255], a[156:159], v[72:75]\n\t"
      "v_add_f32 v234, 1.0, v234\n\t"
      "v_add_f32 v235, 1.0, v235\n\t"
      "v_mfma_f32_16x16x32_bf16 v[72:75], v[212:215], a[160:163], v[72:75]\n\t"
      "v_rcp_f32 v232, v232\n\t"
      "v_mfma_f32_16x16x32_bf16 v[72:75], v[84:87], a[164:167], v[72:75]\n\t"
      "v_rcp_f32 v233, v233\n\t"
      "v_mfma_f32_16x16x32_bf16 v[72:75], v[100:103], a[168:171], v[72:75]\n\t"
      "v_rcp_f32 v234, v234\n\t"
      "v_mfma_f32_16x16x32_bf16 v[72:75], v[116:119], a[172:175], v[72:75]\n\t"
      "v_rcp_f32 v235, v235\n\t"
      "v_mfma_f32_16x16x32_bf16 v[72:75], v[132:135], a[176:179], v[72:75]\n\t"
      "v_fma_f32 v232, v232, s8, v40\n\t"
      "v_fma_f32 v233, v233, s8, v40\n\t"
      "v_mfma_f32_16x16x32_bf16 v[72:75], v[148:151], a[180:183], v[72:75]\n\t"
      "v_fma_f32 v234, v234, s8, v40\n\t"
      "v_fma_f32 v235, v235, s8, v40\n\t"
      "s_waitcnt vmcnt(1)\n\t"
      "v_max3_u32 v42, v204, v205, v206\n\t"
      "s_waitcnt vmcnt(0)\n\t"
      "v_max3_u32 v43, v207, v164, v165\n\t"
      "v_max3_u32 v42, v42, v166, v167\n\t"
      "v_max_u32 v42, v42, v43\n\t"
      "v_cmp_eq_u32 vcc, -1, v42\n\t"
      "s_cbranch_vccnz Lpl%=\n\t"
      "Lplb%=:\n\t"
      "v_fma_f32 v244, v56, s48, v244\n\t"
      "v_fma_f32 v245, v57, s48, v245\n\t"
      "v_fma_f32 v246, v58, s48, v246\n\t"
      "v_fma_f32 v247, v59, s48, v247\n\t"
      "v_exp_f32 v244, v244\n\t"
      "v_exp_f32 v245, v245\n\t"
      "v_exp_f32 v246, v246\n\t"
      "v_exp_f32 v247, v247\n\t"
      "v_add_f32 v244, 1.0, v244\n\t"
      "v_add_f32 v245, 1.0, v245\n\t"
      "v_add_f32 v246, 1.0, v246\n\t"
      "v_add_f32 v247, 1.0, v247\n\t"
      "v_rcp_f32 v244, v244\n\t"
      "v_rcp_f32 v245, v245\n\t"
      "v_rcp_f32 v246, v246\n\t"
      "v_rcp_f32 v247, v247\n\t"
      "v_fma_f32 v244, v244, s8, v40\n\t"
      "v_fma_f32 v245, v245, s8, v40\n\t"
      "v_fma_f32 v246, v246, s8, v40\n\t"
      "v_fma_f32 v247, v247, s8, v40\n\t"
      "v_fma_mix_f32 v152, v152, v204, 0 op_sel:[0,0,0] op_sel_hi:[0,1,0]\n\t"
      "v_fma_mix_f32 v153, v153, v204, 0 op_sel:[0,1,0] op_sel_hi:[0,1,0]\n\t"
      "v_fma_mix_f32 v154, v154, v205, 0 op_sel:[0,0,0] op_sel_hi:[0,1,0]\n\t"
      "v_fma_mix_f32 v155, v155, v205, 0 op_sel:[0,1,0] op_sel_hi:[0,1,0]\n\t"
      "v_cvt_pk_bf16_f32 v156, v152, v153\n\t"
      "v_cvt_pk_bf16_f32 v157, v154, v155\n\t"
      "buffer_store_dwordx2 v[156:157], v28, s[24:27], 0 offen offset:0\n\t"
      "v_fma_mix_f32 v232, v232, v206, 0 op_sel:[0,0,0] op_sel_hi:[0,1,0]\n\t"
      "v_fma_mix_f32 v233, v233, v206, 0 op_sel:[0,1,0] op_sel_hi:[0,1,0]\n\t"
      "v_fma_mix_f32 v234, v234, v207, 0 op_sel:[0,0,0] op_sel_hi:[0,1,0]\n\t"
      "v_fma_mix_f32 v235, v235, v207, 0 op_sel:[0,1,0] op_sel_hi:[0,1,0]\n\t"
      "v_cvt_pk_bf16_f32 v168, v232, v233\n\t"
      "v_cvt_pk_bf16_f32 v169, v234, v235\n\t"
      "buffer_store_dwordx2 v[168:169], v29, s[24:27], 0 offen offset:0\n\t"
      "v_fma_mix_f32 v244, v244, v164, 0 op_sel:[0,0,0] op_sel_hi:[0,1,0]\n\t"
      "v_fma_mix_f32 v245, v245, v164, 0 op_sel:[0,1,0] op_sel_hi:[0,1,0]\n\t"
      "v_fma_mix_f32 v246, v246, v165, 0 op_sel:[0,0,0] op_sel_hi:[0,1,0]\n\t"
      "v_fma_mix_f32 v247, v247, v165, 0 op_sel:[0,1,0] op_sel_hi:[0,1,0]\n\t"
      "v_cvt_pk_bf16_f32 v172, v244, v245\n\t"
      "v_cvt_pk_bf16_f32 v173, v246, v247\n\t"
      "buffer_store_dwordx2 v[172:173], v28, s[24:27], 0 offen offset:32\n\t"
      "v_fma_f32 v224, v72, s48, v224\n\t"
      "v_fma_f32 v225, v73, s48, v225\n\t"
      "v_fma_f32 v226, v74, s48, v226\n\t"
      "v_fma_f32 v227, v75, s48, v227\n\t"
      "v_exp_f32 v224, v224\n\t"
      "v_exp_f32 v225, v225\n\t"
      "v_exp_f32 v226, v226\n\t"
      "v_exp_f32 v227, v227\n\t"
      "v_add_f32 v224, 1.0, v224\n\t"
      "v_add_f32 v225, 1.0, v225\n\t"
      "v_add_f32 v226, 1.0, v226\n\t"
      "v_add_f32 v227, 1.0, v227\n\t"
      "v_rcp_f32 v224, v224\n\t"
      "v_rcp_f32 v225, v225\n\t"
      "v_rcp_f32 v226, v226\n\t"
      "v_rcp_f32 v227, v227\n\t"
      "v_fma_f32 v224, v224, s8, v40\n\t"
      "v_fma_f32 v225, v225, s8, v40\n\t"
      "v_fma_f32 v226, v226, s8, v40\n\t"
      "v_fma_f32 v227, v227, s8, v40\n\t"
      "v_fma_mix_f32 v224, v224, v166, 0 op_sel:[0,0,0] op_sel_hi:[0,1,0]\n\t"
      "v_fma_mix_f32 v225, v225, v166, 0 op_sel:[0,1,0] op_sel_hi:[0,1,0]\n\t"
      "v_fma_mix_f32 v226, v226, v167, 0 op_sel:[0,0,0] op_sel_hi:[0,1,0]\n\t"
      "v_fma_mix_f32 v227, v227, v167, 0 op_sel:[0,1,0] op_sel_hi:[0,1,0]\n\t"
      "v_cvt_pk_bf16_f32 v248, v224, v225\n\t"
      "v_cvt_pk_bf16_f32 v249, v226, v227\n\t"
      "buffer_store_dwordx2 v[248:249], v29, s[24:27], 0 offen offset:32\n\t"
      "s_endpgm\n\t"
      "Lhs%=:\n\t"
      "s_add_u32 s50, s50, 1\n\t"
      "s_cmp_gt_u32 s50, 2097152\n\t"
      "s_cbranch_scc1 Lhsb%=\n\t"
      "buffer_load_dwordx2 v[38:39], off, s[28:31], s47 sc1\n\t"
      "s_waitcnt vmcnt(0)\n\t"
      "v_mov_b32 v36, v38\n\t"
      "buffer_store_dword v38, off, s[28:31], s46 offset:4 sc1\n\t"
      "v_cmp_ne_u32 vcc, v39, v37\n\t"
      "s_nop 1\n\t"
      "s_cbranch_vccz Lhsb%=\n\t"
      "s_sleep 2\n\t"
      "s_branch Lhs%=\n\t"
      "Lpl%=:\n\t"
      "s_add_u32 s51, s51, 1\n\t"
      "s_cmp_gt_u32 s51, 2097152\n\t"
      "s_cbranch_scc1 Lplb%=\n\t"
      "buffer_load_dwordx4 v[204:207], v31, s[28:31], s44 offen offset:0 sc1\n\t"
      "buffer_load_dwordx4 v[164:167], v31, s[28:31], s44 offen offset:1024 sc1\n\t"
      "s_waitcnt vmcnt(0)\n\t"
      "v_max3_u32 v42, v204, v205, v206\n\t"
      "v_max3_u32 v43, v207, v164, v165\n\t"
      "v_max3_u32 v42, v42, v166, v167\n\t"
      "v_max_u32 v42, v42, v43\n\t"
      "v_cmp_eq_u32 vcc, -1, v42\n\t"
      "s_nop 1\n\t"
      "s_cbranch_vccz Lplb%=\n\t"
      "s_branch Lpl%=\n\t"
      "Lce%=:\n\t"
      :
      : "s"(__builtin_amdgcn_kernarg_segment_ptr()), "s"(blockIdx.x), "v"(threadIdx.x), "s"(ldsb)
      : "v1", "v2", "v3", "v4", "v5", "v6", "v7", "v8", "v9", "v10", "v11", "v12", "v13", "v14", "v15", "v16", "v17", "v18", "v19", "v20", "v21", "v22", "v23", "v24", "v25", "v26", "v27", "v28", "v29", "v30", "v31", "v32", "v33", "v34", "v35", "v36", "v37", "v38", "v39", "v40", "v41", "v42", "v43", "v44", "v45", "v46", "v47", "v48", "v49", "v50", "v51", "v52", "v53", "v54", "v55", "v56", "v57", "v58", "v59", "v60", "v61", "v62", "v63", "v64", "v65", "v66", "v67", "v68", "v69", "v70", "v71", "v72", "v73", "v74", "v75", "v76", "v77", "v78", "v79", "v80", "v81", "v82", "v83", "v84", "v85", "v86", "v87", "v88", "v89", "v90", "v91", "v92", "v93", "v94", "v95", "v96", "v97", "v98", "v99", "v100", "v101", "v102", "v103", "v104", "v105", "v106", "v107", "v108", "v109", "v110", "v111", "v112", "v113", "v114", "v115", "v116", "v117", "v118", "v119", "v120", "v121", "v122", "v123", "v124", "v125", "v126", "v127", "v128", "v129", "v130", "v131", "v132", "v133", "v134", "v135", "v136", "v137", "v138", "v139", "v140", "v141", "v142", "v143", "v144", "v145", "v146", "v147", "v148", "v149", "v150", "v151", "v152", "v153", "v154", "v155", "v156", "v157", "v158", "v159", "v160", "v161", "v162", "v163", "v164", "v165", "v166", "v167", "v168", "v169", "v170", "v171", "v172", "v173", "v174", "v175", "v176", "v177", "v178", "v179", "v180", "v181", "v182", "v183", "v184", "v185", "v186", "v187", "v188", "v189", "v190", "v191", "v192", "v193", "v194", "v195", "v196", "v197", "v198", "v199", "v200", "v201", "v202", "v203", "v204", "v205", "v206", "v207", "v208", "v209", "v210", "v211", "v212", "v213", "v214", "v215", "v216", "v217", "v218", "v219", "v220", "v221", "v222", "v223", "v224", "v225", "v226", "v227", "v228", "v229", "v230", "v231", "v232", "v233", "v234", "v235", "v236", "v237", "v238", "v239", "v240", "v241", "v242", "v243", "v244", "v245", "v246", "v247", "v248", "v249", "v250", "v251", "v252", "v253", "v254", "v255", "a0", "a1", "a2", "a3", "a4", "a5", "a6", "a7", "a8", "a9", "a10", "a11", "a12", "a13", "a14", "a15", "a16", "a17", "a18", "a19", "a20", "a21", "a22", "a23", "a24", "a25", "a26", "a27", "a28", "a29", "a30", "a31", "a32", "a33", "a34", "a35", "a36", "a37", "a38", "a39", "a40", "a41", "a42", "a43", "a44", "a45", "a46", "a47", "a48", "a49", "a50", "a51", "a52", "a53", "a54", "a55", "a56", "a57", "a58", "a59", "a60", "a61", "a62", "a63", "a64", "a65", "a66", "a67", "a68", "a69", "a70", "a71", "a72", "a73", "a74", "a75", "a76", "a77", "a78", "a79", "a80", "a81", "a82", "a83", "a84", "a85", "a86", "a87", "a88", "a89", "a90", "a91", "a92", "a93", "a94", "a95", "a96", "a97", "a98", "a99", "a100", "a101", "a102", "a103", "a104", "a105", "a106", "a107", "a108", "a109", "a110", "a111", "a112", "a113", "a114", "a115", "a116", "a117", "a118", "a119", "a120", "a121", "a122", "a123", "a124", "a125", "a126", "a127", "a128", "a129", "a130", "a131", "a132", "a133", "a134", "a135", "a136", "a137", "a138", "a139", "a140", "a141", "a142", "a143", "a144", "a145", "a146", "a147", "a148", "a149", "a150", "a151", "a152", "a153", "a154", "a155", "a156", "a157", "a158", "a159", "a160", "a161", "a162", "a163", "a164", "a165", "a166", "a167", "a168", "a169", "a170", "a171", "a172", "a173", "a174", "a175", "a176", "a177", "a178", "a179", "a180", "a181", "a182", "a183", "a184", "a185", "a186", "a187", "a188", "a189", "a190", "a191", "a192", "a193", "a194", "a195", "a196", "a197", "a198", "a199", "a200", "a201", "a202", "a203", "a204", "a205", "a206", "a207", "a208", "a209", "a210", "a211", "a212", "a213", "a214", "a215", "a216", "a217", "a218", "a219", "a220", "a221", "a222", "a223", "a224", "a225", "a226", "a227", "a228", "a229", "a230", "a231", "a232", "a233", "a234", "a235", "a236", "a237", "a238", "a239", "a240", "a241", "a242", "a243", "a244", "a245", "a246", "a247", "a248", "a249", "a250", "a251", "a252", "a253", "a254", "a255", "s8", "s9", "s10", "s11", "s12", "s13", "s14", "s15", "s16", "s17", "s18", "s19", "s20", "s21", "s22", "s23", "s24", "s25", "s26", "s27", "s28", "s29", "s30", "s31", "s32", "s33", "s34", "s35", "s36", "s37", "s38", "s39", "s40", "s41", "s42", "s43", "s44", "s45", "s46", "s47", "s48", "s49", "s50", "s51", "s52", "s53", "s54", "s55", "s56", "s57", "s58", "s59", "s60", "s61", "s62", "s63", "s64", "s65", "s66", "s67", "s68", "s69", "s70", "s71", "s72", "s73", "s74", "s75", "s76", "s77", "s78", "s79", "s80", "s81", "s82", "s83", "s84", "s85", "s86", "s87", "s88", "s89", "s90", "s91", "s92", "s93", "s94", "s95", "s96", "s97", "s98", "s99", "s100", "s101", "m0", "scc", "vcc", "memory");
}

extern "C" void kernel_launch(void* const* d_in, const int* in_sizes, int n_in,
                              void* d_out, int out_size, void* d_ws, size_t ws_size,
                              hipStream_t stream) {
  conv_gate_kernel<<<256, 256, 0, stream>>>((const __hip_bfloat16*)d_in[0], (const __hip_bfloat16*)d_in[1],
                                            (__hip_bfloat16*)d_out, (unsigned*)d_ws);
}
```
